```python
import torch

def _softmax(x, axis=-1):
    return torch.softmax(x, dim=axis)
def _gelu(x):
    return torch.nn.functional.gelu(x, approximate="tanh")
def _silu(x):
    return torch.nn.functional.silu(x)
def _sigmoid(x):
    return torch.sigmoid(x)
def _layernorm(x, axis=-1, eps=1e-5):
    m = x.mean(dim=axis, keepdim=True)
    v = x.var(dim=axis, keepdim=True, unbiased=False)
    return (x - m) / torch.sqrt(v + eps)
def _scatter_add(tgt, idx, src, axis=0):
    return tgt.clone().index_add_(axis, idx, src)
def _segment_sum(src, seg, n):
    out = torch.zeros((n,) + src.shape[1:], dtype=src.dtype)
    return out.index_add_(0, seg, src)
def _pad(x, pw):
    flat = tuple(v for pair in reversed(pw) for v in pair)
    return torch.nn.functional.pad(x, flat)

kernel_name = 'standalone__linear_kernel_hedgehog__prompt'

def setup_inputs():

    _w = (lambda a: (lambda t: t.to(torch.bfloat16) if t.is_floating_point() else t)(torch.as_tensor(a)))
    B, S, D, H = 1, 2048, 640, 32
    Dh = D // H
    return {
        "x": _w((lambda *s: torch.randn(*s, dtype=torch.bfloat16) * 0.4)(B, S, D)),
        "wqkv": _w((lambda *s: torch.randn(*s, dtype=torch.bfloat16) * 0.4)(D, 3 * D)),
        "w_phi": _w((lambda *s: torch.randn(*s, dtype=torch.bfloat16) * 0.4)(H, Dh, Dh) * (Dh ** -0.5)),
        "wo": _w((lambda *s: torch.randn(*s, dtype=torch.bfloat16) * 0.05)(D, D)),
    }

def reference(x, wqkv, w_phi, wo):
    B, S, D = x.shape
    H = 32; Dh = D // H
    qkv = (lambda x, *d: x.permute(*d))((x @ wqkv).reshape(B, S, 3, H, Dh), 2, 0, 3, 1, 4)
    q, k, v = qkv[0], qkv[1], qkv[2]
    pq = torch.einsum("bhsd,hdf->bhsf", q, w_phi)
    pk = torch.einsum("bhsd,hdf->bhsf", k, w_phi)
    phi_q = torch.concatenate([_softmax(pq, axis=-1), _softmax(-pq, axis=-1)], axis=-1)
    phi_k = torch.concatenate([_softmax(pk, axis=-1), _softmax(-pk, axis=-1)], axis=-1)
    kv = torch.einsum("bhsf,bhsd->bhfd", phi_k, v)
    z  = torch.einsum("bhsf->bhf", phi_k)
    num = torch.einsum("bhqf,bhfd->bhqd", phi_q, kv)
    den = torch.einsum("bhqf,bhf->bhq", phi_q, z)[..., None] + 1e-6
    o = num / den
    return (lambda x, *d: x.permute(*d))(o, 0, 2, 1, 3).reshape(B, S, D) @ wo
```

```cpp
#include <hip/hip_runtime.h>
#include <hip/hip_bf16.h>
typedef __hip_bfloat16 bf16;
constexpr int S=2048,D=640,H=32,DH=20;
__device__ inline float b2f(bf16 v){return __bfloat162float(v);}
__device__ inline float rb(float v){return __bfloat162float(__float2bfloat16(v));}

__global__ void k_gemm(const bf16* A,const bf16* B,bf16* C,int M,int N,int K){
  int n=blockIdx.x*64+(threadIdx.x&63); int m=blockIdx.y*4+(threadIdx.x>>6);
  if(n>=N||m>=M)return; float acc=0;
  for(int k=0;k<K;k++)acc+=b2f(A[m*K+k])*b2f(B[k*N+n]);
  C[m*N+n]=__float2bfloat16(acc);
}
__global__ __launch_bounds__(256) void k_phi(const bf16* qkv,const bf16* wphi,float* phiq,float* phik){
  int idx=blockIdx.x*256+threadIdx.x; if(idx>=S*H*2)return;
  int which=idx/(S*H); int r=idx%(S*H); int s=r/H,h=r%H;
  float p[DH];
  #pragma unroll
  for(int f=0;f<DH;f++){float a=0;
    #pragma unroll
    for(int d=0;d<DH;d++)a+=b2f(qkv[s*3*D+which*D+h*DH+d])*b2f(wphi[h*DH*DH+d*DH+f]);
    p[f]=rb(a);}
  float mx=-1e30f,mn=1e30f;
  #pragma unroll
  for(int f=0;f<DH;f++){mx=fmaxf(mx,p[f]);mn=fminf(mn,p[f]);}
  float s1=0,s2=0;
  #pragma unroll
  for(int f=0;f<DH;f++){s1+=__expf(p[f]-mx);s2+=__expf(mn-p[f]);}
  float* o=(which?phik:phiq)+(size_t)(s*H+h)*40;
  #pragma unroll
  for(int f=0;f<DH;f++){o[f]=rb(__expf(p[f]-mx)/s1);o[20+f]=rb(__expf(mn-p[f])/s2);}
}
__global__ void k_kv(const bf16* qkv,const float* phik,float* kv){
  int idx=blockIdx.x*256+threadIdx.x; if(idx>=H*40*21)return;
  int h=idx/(40*21),r=idx%(40*21),f=r/21,d=r%21; float a=0;
  for(int s=0;s<S;s++){float v=d<20?b2f(qkv[s*3*D+2*D+h*DH+d]):1.f; a+=phik[(size_t)(s*H+h)*40+f]*v;}
  kv[idx]=rb(a);
}
__global__ void k_o(const float* phiq,const float* kv,bf16* o){
  int idx=blockIdx.x*256+threadIdx.x; if(idx>=S*D)return;
  int s=idx/D,c=idx%D,h=c/DH,d=c%DH; float n=0,den=0;
  for(int f=0;f<40;f++){float p=phiq[(size_t)(s*H+h)*40+f]; n+=p*kv[h*840+f*21+d]; den+=p*kv[h*840+f*21+20];}
  o[idx]=__float2bfloat16(rb(n)/rb(rb(den)+1e-6f));
}
extern "C" void kernel_launch(void* const* d_in,const int* in_sizes,int n_in,void* d_out,int out_size,void* d_ws,size_t ws_size,hipStream_t stream){
  const bf16* x=(const bf16*)d_in[0];const bf16* wqkv=(const bf16*)d_in[1];const bf16* wphi=(const bf16*)d_in[2];const bf16* wo=(const bf16*)d_in[3];
  char* ws=(char*)d_ws;
  bf16* qkv=(bf16*)ws; float* phiq=(float*)(ws+(16<<20)); float* phik=(float*)(ws+(32<<20)); float* kv=(float*)(ws+(48<<20)); bf16* o=(bf16*)(ws+(50<<20));
  k_gemm<<<dim3(3*D/64,S/4),256,0,stream>>>(x,wqkv,qkv,S,3*D,D);
  k_phi<<<S*H*2/256,256,0,stream>>>(qkv,wphi,phiq,phik);
  k_kv<<<(H*840+255)/256,256,0,stream>>>(qkv,phik,kv);
  k_o<<<S*D/256,256,0,stream>>>(phiq,kv,o);
  k_gemm<<<dim3(D/64,S/4),256,0,stream>>>(o,wo,(bf16*)d_out,S,D,D);
}
```

```cpp
#include <hip/hip_runtime.h>
#include <hip/hip_bf16.h>
#include <stdint.h>

#define MIX 1

namespace hh {

typedef unsigned u32;
typedef unsigned long long u64;
typedef u32 u32x2 __attribute__((ext_vector_type(2)));
typedef u32 u32x4 __attribute__((ext_vector_type(4)));
typedef short s16x4 __attribute__((ext_vector_type(4)));
typedef __bf16 bf16x2 __attribute__((ext_vector_type(2)));
typedef __bf16 bf16x8 __attribute__((ext_vector_type(8)));
typedef float f32x2 __attribute__((ext_vector_type(2)));
typedef float f32x4 __attribute__((ext_vector_type(4)));
typedef float f32x16 __attribute__((ext_vector_type(16)));
#define LAS __attribute__((address_space(3)))
#define DI __device__ __forceinline__

constexpr int R0 = 0, R1 = 51200, R2 = 102400, R3 = 153600;
constexpr int L_DUMP = R3, L_WPH = R3 + 2048, L_ZERO = L_WPH + 3200, L_KV = R3 + 6144;
constexpr int L_PHI = R0, L_V = R0 + 20480;
constexpr int LDS_BYTES = 163840;

constexpr u32 W_EPOCH = 0, W_F1 = 4096, W_F2 = 8192, W_PART = 16384, PSTR = 3456;
constexpr u32 W_O = W_PART + 256 * PSTR;
constexpr u32 W_END = W_O + 128 * 80 * 256;
constexpr u32 W_STAMP = 8u << 20;
constexpr u32 OOB = 0x7ffffff0u;
constexpr u32 POISON = 0xffffffffu;
constexpr int SPIN_CAP = 1 << 18;

DI u32x4 make_srd(const void* p, u32 bytes) {
  const u64 a = (u64)p;
  return u32x4{(u32)a, (u32)(a >> 32) & 0xffffu, bytes, 0x00020000u};
}
DI void glds(u32 lds, u32 voff, u32x4 srd, u32 soff) {
  asm volatile("s_mov_b32 m0, %0\n\ts_nop 0\n\tbuffer_load_dwordx4 %1, %2, %3 offen lds"
               :: "s"(lds), "v"(voff), "s"(srd), "s"(soff) : "memory");
}
DI u32x4 gload16_sc1(u32 voff, u32x4 srd, u32 soff) {
  u32x4 r;
  asm volatile("buffer_load_dwordx4 %0, %1, %2, %3 offen sc1" : "=v"(r) : "v"(voff), "s"(srd), "s"(soff) : "memory");
  return r;
}
DI u32x2 gload8_sc1(u32 voff, u32x4 srd, u32 soff) {
  u32x2 r;
  asm volatile("buffer_load_dwordx2 %0, %1, %2, %3 offen sc1" : "=v"(r) : "v"(voff), "s"(srd), "s"(soff) : "memory");
  return r;
}
DI u32 gload4_sc1(u32 voff, u32x4 srd, u32 soff) {
  u32 r;
  asm volatile("buffer_load_dword %0, %1, %2, %3 offen sc1" : "=v"(r) : "v"(voff), "s"(srd), "s"(soff) : "memory");
  return r;
}
DI void gstore16(u32x4 d, u32 voff, u32x4 srd, u32 soff) {
  asm volatile("buffer_store_dwordx4 %0, %1, %2, %3 offen\n\ts_nop 1" :: "v"(d), "v"(voff), "s"(srd), "s"(soff) : "memory");
}
DI void gstore8(u32x2 d, u32 voff, u32x4 srd, u32 soff) {
  asm volatile("buffer_store_dwordx2 %0, %1, %2, %3 offen" :: "v"(d), "v"(voff), "s"(srd), "s"(soff) : "memory");
}
DI void gstore16_sc1(u32x4 d, u32 voff, u32x4 srd, u32 soff) {
  asm volatile("buffer_store_dwordx4 %0, %1, %2, %3 offen sc1\n\ts_nop 1" :: "v"(d), "v"(voff), "s"(srd), "s"(soff) : "memory");
}
DI void gstore8_sc1(u32x2 d, u32 voff, u32x4 srd, u32 soff) {
  asm volatile("buffer_store_dwordx2 %0, %1, %2, %3 offen sc1" :: "v"(d), "v"(voff), "s"(srd), "s"(soff) : "memory");
}
#define SCHED_FENCE() __builtin_amdgcn_sched_barrier(0)
template <int N> DI void wait_vm() { asm volatile("s_waitcnt vmcnt(%0)" :: "n"(N) : "memory"); }
template <int N> DI void wait_vm_bar() { asm volatile("s_waitcnt vmcnt(%0)\n\ts_barrier" :: "n"(N) : "memory"); }
template <int N> DI void wait_lgkm() { asm volatile("s_waitcnt lgkmcnt(%0)" :: "n"(N) : "memory"); }
DI void lds_bar() { asm volatile("s_waitcnt lgkmcnt(0)\n\ts_barrier" ::: "memory"); }
DI void mem_order() { asm volatile("" ::: "memory"); }

DI u32x2 tr_read(u32 addr) {
  return __builtin_bit_cast(u32x2, __builtin_amdgcn_ds_read_tr16_b64_v4i16((LAS s16x4*)(size_t)addr));
}
DI u32x4 tr_frag(u32 a0, u32 a1) {
  const u32x2 lo = tr_read(a0), hi = tr_read(a1);
  return u32x4{lo[0], lo[1], hi[0], hi[1]};
}
DI u32x4 lds_read16(u32 addr) { return *(LAS u32x4*)(size_t)addr; }
DI void lds_write16(u32 addr, u32x4 v) { *(LAS u32x4*)(size_t)addr = v; }
DI void lds_write8(u32 addr, u32x2 v) { *(LAS u32x2*)(size_t)addr = v; }
DI f32x4 mfma16(u32x4 a, u32x4 b, f32x4 c) {
  return __builtin_amdgcn_mfma_f32_16x16x32_bf16(__builtin_bit_cast(bf16x8, a), __builtin_bit_cast(bf16x8, b), c, 0, 0, 0);
}
DI f32x16 mfma32(u32x4 a, u32x4 b, f32x16 c) {
  return __builtin_amdgcn_mfma_f32_32x32x16_bf16(__builtin_bit_cast(bf16x8, a), __builtin_bit_cast(bf16x8, b), c, 0, 0, 0);
}
DI u32 pack2(float a, float b) { return __builtin_bit_cast(u32, __builtin_convertvector(f32x2{a, b}, bf16x2)); }
DI u32 nopoison(u32 x) { return x < POISON - 1 ? x : POISON - 1; }
DI void swap16(u32& a, u32& b) {
  const auto r = __builtin_amdgcn_permlane16_swap(a, b, false, false);
  a = r[0]; b = r[1];
}

#define STAMP(i) do {} while (0)

constexpr int r_need(int c) { return (5 * c + 4) / 4; }
constexpr int g_size(int c) { return c < 10 ? 4 + r_need(c) - (c ? r_need(c - 1) : -1) : (c == 10 ? 5 : 4); }

struct Ctx {
  u32x4 sX, sW, sWo, sWs, sOut;
  u32 vw[5];
  int w, rot;
};

template <int R> DI void issue_w(const Ctx& c, u32 region, u32 colbyte) {
  const int i = 4 * R + c.w;
  int kc = i / 5 + c.rot;
  kc = kc >= 10 ? kc - 10 : kc;
  u32 soff = (u32)kc * (64u * 3840u) + colbyte, lds = region + (u32)i * 1024u;
  if (R == 12) { soff = i >= 50 ? OOB : soff; lds = i >= 50 ? (u32)L_DUMP + (u32)(i - 50) * 1024u : lds; }
  glds(lds, c.vw[R % 5], c.sW, soff);
}
template <int R0_, int R1_> DI void issue_w_range(const Ctx& c, u32 region, u32 colbyte) {
  if constexpr (R0_ <= R1_) { issue_w<R0_>(c, region, colbyte); issue_w_range<R0_ + 1, R1_>(c, region, colbyte); }
}
template <int C> DI void issue_x(const Ctx& c, const u32 (&vx)[2], u32 rowbyte) {
  int kc = C + c.rot;
  kc = kc >= 10 ? kc - 10 : kc;
#pragma unroll
  for (int u = 0; u < 4; ++u)
    glds(R2 + (C % 3) * 16384 + c.w * 4096 + u * 1024, vx[u & 1], c.sX, rowbyte + (u32)kc * 128u + (u >> 1) * 20480u);
}

template <int KS> DI void gemm_step(const u32 (&ta)[3], const u32x4 (&xf)[20][2], f32x4 (&acc)[3][2]) {
#pragma unroll
  for (int t = 0; t < 3; ++t) {
    const u32x4 a = tr_frag(ta[t] + KS * 2560, ta[t] + KS * 2560 + 80);
    acc[t][0] = mfma16(a, xf[KS][0], acc[t][0]);
    acc[t][1] = mfma16(a, xf[KS][1], acc[t][1]);
  }
}

DI void acc_to_rows(const f32x4 (&acc)[3][2], u32 (&p)[10]) {
#pragma unroll
  for (int t = 0; t < 3; ++t)
#pragma unroll
    for (int i = 0; i < 2; ++i) {
      u32 a = pack2(acc[t][0][2 * i], acc[t][0][2 * i + 1]), b = pack2(acc[t][1][2 * i], acc[t][1][2 * i + 1]);
      swap16(a, b);
      p[4 * t + i] = a;
      if (t < 2) p[4 * t + 2 + i] = b;
    }
}

DI void load_wphi(u32 pairbase, int lane, u32x4 (&wf)[2][3]) {
  const int hh = lane >> 5, gi = (lane >> 4) & 1, q = (lane >> 2) & 3, p = lane & 3;
  const bool mine = (p & 1) == hh;
  const u32 row = pairbase + hh * 800 + q * 40;
#pragma unroll
  for (int mt = 0; mt < 2; ++mt) {
    const bool ok = mine && (mt == 0 || (gi == 0 && p < 2));
    const u32 col = mt == 0 ? (2 * gi + (p >> 1)) * 8 : 32;
    const u32 b = ok ? row + col + hh * 160 : (u32)L_ZERO;
    const u32 b4 = ok ? row + col + (hh ? 0 : 640) : (u32)L_ZERO;
    wf[mt][0] = tr_frag(b, b + 160);
    wf[mt][1] = tr_frag(b + 320, b + 480);
    const u32x2 l = tr_read(b4);
    wf[mt][2] = u32x4{l[0], l[1], 0u, 0u};
  }
}

DI void feature_map(const u32 (&p)[10], const u32x4 (&wf)[2][3], u32 (&phi)[20]) {
  f32x16 z;
#pragma unroll
  for (int i = 0; i < 16; ++i) z[i] = 0.f;
  const u32x4 b0 = {p[0], p[1], p[2], p[3]}, b1 = {p[4], p[5], p[6], p[7]}, b2 = {p[8], p[9], 0u, 0u};
  f32x16 d0 = mfma32(wf[0][0], b0, z), d1 = mfma32(wf[1][0], b0, z);
  d0 = mfma32(wf[0][1], b1, d0); d1 = mfma32(wf[1][1], b1, d1);
  d0 = mfma32(wf[0][2], b2, d0); d1 = mfma32(wf[1][2], b2, d1);
  float v[20];
#pragma unroll
  for (int i = 0; i < 16; ++i) v[i] = d0[i];
#pragma unroll
  for (int i = 0; i < 4; ++i) v[16 + i] = d1[i];
  float mx = fmaxf(v[0], v[1]), mn = fminf(v[0], v[1]);
#pragma unroll
  for (int i = 2; i < 20; i += 2) { mx = fmaxf(fmaxf(mx, v[i]), v[i + 1]); mn = fminf(fminf(mn, v[i]), v[i + 1]); }
  constexpr float L2E = 1.4426950408889634f;
  const float cx = -mx * L2E, cn = mn * L2E;
  f32x2 ep[10], en[10];
#pragma unroll
  for (int i = 0; i < 10; ++i) {
    const f32x2 x = {v[2 * i], v[2 * i + 1]};
    const f32x2 a = __builtin_elementwise_fma(x, f32x2{L2E, L2E}, f32x2{cx, cx});
    const f32x2 b = __builtin_elementwise_fma(x, f32x2{-L2E, -L2E}, f32x2{cn, cn});
    ep[i] = f32x2{__builtin_amdgcn_exp2f(a[0]), __builtin_amdgcn_exp2f(a[1])};
    en[i] = f32x2{__builtin_amdgcn_exp2f(b[0]), __builtin_amdgcn_exp2f(b[1])};
  }
  f32x2 sp = ep[0], sn = en[0];
#pragma unroll
  for (int i = 1; i < 10; ++i) { sp += ep[i]; sn += en[i]; }
  const float rp = __builtin_amdgcn_rcpf(sp[0] + sp[1]), rn = __builtin_amdgcn_rcpf(sn[0] + sn[1]);
#pragma unroll
  for (int i = 0; i < 10; ++i) {
    const f32x2 a = ep[i] * f32x2{rp, rp}, b = en[i] * f32x2{rn, rn};
    phi[i] = pack2(a[0], a[1]);
    phi[10 + i] = pack2(b[0], b[1]);
  }
}

template <int C> DI void pass1_iter(const Ctx& c, const u32 (&vx)[2], const u32 (&xa)[2], const u32 (&ta)[3], u32 rowbyte, u32 colk, u32 colv,
                                    u32x4 (&xf)[20][2], f32x4 (&acc)[3][2]) {
  wait_vm_bar<g_size(C + 1) + g_size(C + 2)>();
#pragma unroll
  for (int kk = 0; kk < 2; ++kk)
#pragma unroll
    for (int st = 0; st < 2; ++st) xf[2 * C + kk][st] = lds_read16(xa[kk] + (C % 3) * 16384 + st * 2048);
  wait_lgkm<0>();
  if constexpr (C + 3 < 10) {
    issue_x<C + 3>(c, vx, rowbyte);
    issue_w_range<r_need(C + 2) + 1, r_need(C + 3)>(c, R0, colk);
  } else if constexpr (C == 7) issue_w_range<0, 4>(c, R1, colv);
  else if constexpr (C == 8) issue_w_range<5, 8>(c, R1, colv);
  else issue_w_range<9, 12>(c, R1, colv);
  gemm_step<2 * C>(ta, xf, acc);
  gemm_step<2 * C + 1>(ta, xf, acc);
  SCHED_FENCE();
}
template <int C> DI void pass1_range(const Ctx& c, const u32 (&vx)[2], const u32 (&xa)[2], const u32 (&ta)[3], u32 rowbyte, u32 colk, u32 colv,
                                     u32x4 (&xf)[20][2], f32x4 (&acc)[3][2]) {
  if constexpr (C < 10) { pass1_iter<C>(c, vx, xa, ta, rowbyte, colk, colv, xf, acc); pass1_range<C + 1>(c, vx, xa, ta, rowbyte, colk, colv, xf, acc); }
}
template <int C, int AFTER> DI void pass_range(const u32 (&ta)[3], const u32x4 (&xf)[20][2], f32x4 (&acc)[3][2]) {
  if constexpr (C < 10) {
    wait_vm_bar<AFTER + 12 - r_need(C)>();
    gemm_step<2 * C>(ta, xf, acc);
    gemm_step<2 * C + 1>(ta, xf, acc);
    SCHED_FENCE();
    pass_range<C + 1, AFTER>(ta, xf, acc);
  }
}
template <int R> DI void issue_wo(const Ctx& c, const u32 (&vo)[5], u32 colbyte) {
  const int i = 4 * R + c.w;
  const u32 lds = i < 50 ? R1 + i * 1024 : R0 + (i - 50) * 1024;
  glds(lds, vo[R % 5], c.sWo, (u32)(i / 5) * (32u * 1280u) + colbyte);
}
template <int A, int B> DI void issue_wo_range(const Ctx& c, const u32 (&vo)[5], u32 colbyte) {
  if constexpr (A <= B) { issue_wo<A>(c, vo, colbyte); issue_wo_range<A + 1, B>(c, vo, colbyte); }
}

__global__ __launch_bounds__(256, 1) void hedgehog_fused(const void* __restrict__ x, const void* __restrict__ wqkv, const void* __restrict__ wphi,
                                                         const void* __restrict__ wo, void* __restrict__ out, void* __restrict__ ws) {
  STAMP(0);
  __shared__ __attribute__((aligned(16))) char smem[LDS_BYTES];
  asm volatile("" :: "v"((u32)(size_t)(LAS char*)smem));
  const int lane = threadIdx.x & 63;
  const int w = __builtin_amdgcn_readfirstlane(threadIdx.x >> 6);
  const int b = blockIdx.x, xg = b & 7, j = b >> 3;
  const int rt = (xg >> 1) * 4 + (j >> 3), hp = (xg & 1) * 8 + (j & 7), hq = hp ^ MIX;
  const int r64 = xg * 4 + (j >> 3), ct = j & 7;
  Ctx c;
  c.sX = make_srd(x, 2048 * 1280); c.sW = make_srd(wqkv, 640 * 3840); c.sWo = make_srd(wo, 640 * 1280);
  c.sWs = make_srd(ws, W_END); c.sOut = make_srd(out, 2048 * 1280);
  const u32x4 sPhi = make_srd(wphi, 25600);
  c.w = w; c.rot = (j + 3 * xg) % 10;
  const int g = lane >> 4, q = (lane >> 2) & 3, p = lane & 3, s16 = lane & 15, hh = lane >> 5, s32 = lane & 31;

  u32 vx[2];
#pragma unroll
  for (int u = 0; u < 2; ++u) { const int r = u * 8 + (lane >> 3); vx[u] = (w * 32 + r) * 1280 + (((lane & 7) ^ (r >> 1)) << 4); }
#pragma unroll
  for (int i = 0; i < 5; ++i) {
    const int cc = ((4 * i + w) % 5) * 64 + lane, rho = cc / 5, pc = cc - rho * 5;
    const int k = (rho & 48) + ((rho >> 3) & 1) * 8 + (rho & 1) * 4 + ((rho >> 1) & 3);
    c.vw[i] = k * 3840 + pc * 16;
  }
  const u32 rowbyte = rt * (128 * 1280), colq = hq * 80, colk = 1280 + hp * 80, colv = 2560 + hp * 80;

  u32x2 ep = gload8_sc1(0u, c.sWs, W_EPOCH);
  { const int cc = w * 64 + lane; glds(L_WPH + w * 1024, cc < 200 ? cc * 16 : OOB, sPhi, (hp >> 1) * 3200); }
  {
    const u32x4 ps = {POISON, POISON, POISON, POISON};
    const int cc = w * 64 + lane;
    gstore16_sc1(ps, cc < 210 ? cc * 16 : OOB, c.sWs, W_PART + (hp * 16 + rt) * PSTR);
#pragma unroll
    for (int i = 0; i < 3; ++i) {
      const int e = i * 64 + lane, st = e >= 80, r = e - st * 80;
      gstore16_sc1(ps, e < 160 ? st * (80 * 256) + r * 16 : OOB, c.sWs, W_O + ((rt * 8 + w * 2) * 80 + hq * 5) * 256);
    }
  }
  issue_x<0>(c, vx, rowbyte); issue_w_range<0, 1>(c, R0, colk);
  issue_x<1>(c, vx, rowbyte); issue_w_range<2, 2>(c, R0, colk);
  issue_x<2>(c, vx, rowbyte); issue_w_range<3, 3>(c, R0, colk);

  u32 xa[2], ta[3];
  xa[0] = R2 + w * 4096 + s16 * 128 + ((g ^ (s16 >> 1)) << 4);
  xa[1] = xa[0] ^ 64;
  const u32 trow = 16 * (g >> 1) + 8 * (g & 1) + 2 * q;
  ta[0] = R0 + trow * 80 + (p < 2 ? p : p + 4) * 8;
  ta[1] = ta[0] + 16;
  ta[2] = R0 + trow * 80 + (4 + (p >> 1)) * 8;

  asm volatile("s_waitcnt vmcnt(%1)\n\ts_barrier" : "+v"(ep) : "n"(g_size(0) + g_size(1) + g_size(2)) : "memory");
  const u64 epoch = ((u64)(u32)__builtin_amdgcn_readfirstlane(ep[1]) << 32) | (u32)__builtin_amdgcn_readfirstlane(ep[0]);
  const u64 tag = epoch * 6364136223846793005ull + 1442695040888963407ull;
  const u32x2 tagv = {(u32)tag, (u32)(tag >> 32)};
  u32x4 wf[2][3];
  load_wphi(L_WPH + (hp & 1) * 1600, lane, wf);
  STAMP(1);

  u32x4 xf[20][2];
  f32x4 acc[3][2];
#pragma unroll
  for (int t = 0; t < 3; ++t) { acc[t][0] = f32x4{0, 0, 0, 0}; acc[t][1] = f32x4{0, 0, 0, 0}; }
  pass1_range<0>(c, vx, xa, ta, rowbyte, colk, colv, xf, acc);
  STAMP(2);
  lds_bar();
  issue_w_range<0, 12>(c, R2, colq);
  gstore8_sc1(tagv, (w == 0 && lane < 2) ? (lane ? W_F2 + (rt * 16 + hq) * 8 : W_F1 + (hp * 16 + rt) * 8) : OOB, c.sWs, 0u);

  {
    u32 pk[10], phi[20];
    acc_to_rows(acc, pk);
    feature_map(pk, wf, phi);
    const u32 a = L_PHI + (hh * 128 + w * 32 + s32) * 80;
#pragma unroll
    for (int i = 0; i < 5; ++i) lds_write16(a + i * 16, u32x4{phi[4 * i], phi[4 * i + 1], phi[4 * i + 2], phi[4 * i + 3]});
  }
  STAMP(3);
#pragma unroll
  for (int t = 0; t < 3; ++t) { acc[t][0] = f32x4{0, 0, 0, 0}; acc[t][1] = f32x4{0, 0, 0, 0}; ta[t] += R1 - R0; }
  pass_range<0, 14>(ta, xf, acc);
  STAMP(4);
  {
    u32 pv[10];
    acc_to_rows(acc, pv);
    const u32 a = L_V + (hh * 128 + w * 32 + s32) * 48;
    lds_write16(a, u32x4{pv[0], pv[1], pv[2], pv[3]});
    lds_write16(a + 16, u32x4{pv[4], pv[5], pv[6], pv[7]});
    lds_write16(a + 32, u32x4{pv[8], pv[9], 0x3f80u, 0u});
  }
  lds_bar();
  {
    const int head = w >> 1, nt = w & 1;
    const u32 pa = L_PHI + (head * 128 + trow) * 80 + p * 8, pa2 = L_PHI + (head * 128 + trow) * 80 + (8 + (p & 1)) * 8;
    const u32 pb = L_V + (head * 128 + trow) * 48 + (nt ? 4 + (p & 1) : p) * 8;
    f32x4 d[3] = {f32x4{0, 0, 0, 0}, f32x4{0, 0, 0, 0}, f32x4{0, 0, 0, 0}};
#pragma unroll
    for (int ks = 0; ks < 4; ++ks) {
      const u32x4 bf = tr_frag(pb + ks * 1536, pb + ks * 1536 + 48);
      d[0] = mfma16(tr_frag(pa + ks * 2560, pa + ks * 2560 + 80), bf, d[0]);
      d[1] = mfma16(tr_frag(pa + ks * 2560 + 32, pa + ks * 2560 + 112), bf, d[1]);
      d[2] = mfma16(tr_frag(pa2 + ks * 2560, pa2 + ks * 2560 + 80), bf, d[2]);
    }
    u32 a0 = pack2(d[0][0], d[0][1]), a1 = pack2(d[0][2], d[0][3]), b0 = pack2(d[1][0], d[1][1]), b1 = pack2(d[1][2], d[1][3]);
    swap16(a0, b0); swap16(a1, b1);
    const bool ok = nt == 0 || s16 < 5;
    const u32 rowoff = (head * 21 + nt * 16 + s16) * 80;
    const u32 so = W_PART + (hp * 16 + rt) * PSTR;
    gstore16_sc1(u32x4{nopoison(a0), nopoison(a1), nopoison(b0), nopoison(b1)}, ok ? rowoff + (g & 1) * 32 + (g >> 1) * 16 : OOB, c.sWs, so);
    gstore8_sc1(u32x2{nopoison(pack2(d[2][0], d[2][1])), nopoison(pack2(d[2][2], d[2][3]))}, (ok && g < 2) ? rowoff + 64 + g * 8 : OOB, c.sWs, so);
  }
  STAMP(5);
  u32 vo[5];
#pragma unroll
  for (int i = 0; i < 5; ++i) {
    const int cc = ((4 * i + w) % 5) * 64 + lane, rho = cc / 10, pc = cc - rho * 10;
    const int k = (rho & 16) + ((rho >> 2) & 1) * 8 + ((rho >> 3) & 1) * 4 + (rho & 3);
    vo[i] = k * 1280 + pc * 16;
  }
  const u32 flagoff = lane < 16 ? W_F1 + (hq * 16 + lane) * 8 : (lane < 32 ? W_F2 + ((r64 >> 1) * 16 + lane - 16) * 8 : OOB);
  u32x2 fl = gload8_sc1(flagoff, c.sWs, 0u);
  issue_wo_range<0, 11>(c, vo, ct * 160);
#pragma unroll
  for (int t = 0; t < 3; ++t) { acc[t][0] = f32x4{0, 0, 0, 0}; acc[t][1] = f32x4{0, 0, 0, 0}; ta[t] += R2 - R1; }
  pass_range<0, 16>(ta, xf, acc);
  STAMP(6);

  asm volatile("s_waitcnt vmcnt(12)" : "+v"(fl) :: "memory");
  for (int s = 0; s < SPIN_CAP; ++s) {
    if (__all(lane >= 32 || (fl[0] == tagv[0] && fl[1] == tagv[1]))) break;
    fl = gload8_sc1(flagoff, c.sWs, 0u);
    asm volatile("s_waitcnt vmcnt(0)" : "+v"(fl) :: "memory");
  }
  u32 goff[4];
#pragma unroll
  for (int a = 0; a < 4; ++a) { const int ch = (w + 4 * a) * 16 + s16; goff[a] = ch < 210 ? g * PSTR + ch * 16 : OOB; }
  u32x4 gl[4][4];
#pragma unroll
  for (int a = 0; a < 4; ++a)
#pragma unroll
    for (int e = 0; e < 4; ++e) gl[a][e] = gload16_sc1(goff[a], c.sWs, W_PART + (hq * 16 + 4 * e) * PSTR);
  issue_wo_range<12, 24>(c, vo, ct * 160);

  u32 phq[20];
  {
    if (MIX) load_wphi(L_WPH + (hq & 1) * 1600, lane, wf);
    u32 pq[10];
    acc_to_rows(acc, pq);
    feature_map(pq, wf, phq);
  }
  SCHED_FENCE();
  STAMP(7);
#define GL_ALL "+v"(gl[0][0]), "+v"(gl[0][1]), "+v"(gl[0][2]), "+v"(gl[0][3]), "+v"(gl[1][0]), "+v"(gl[1][1]), "+v"(gl[1][2]), "+v"(gl[1][3]), \
               "+v"(gl[2][0]), "+v"(gl[2][1]), "+v"(gl[2][2]), "+v"(gl[2][3]), "+v"(gl[3][0]), "+v"(gl[3][1]), "+v"(gl[3][2]), "+v"(gl[3][3])
  asm volatile("s_waitcnt vmcnt(13)" : GL_ALL :: "memory");
  {
    const u32 one = 0x3f80u << ((s16 & 1) * 16);
    const u32x4 sel = {(s16 >> 1) == 0 ? one : 0u, (s16 >> 1) == 1 ? one : 0u, (s16 >> 1) == 2 ? one : 0u, (s16 >> 1) == 3 ? one : 0u};
    f32x4 r[4];
    for (int s = 0;; ++s) {
      float chk = 0.f;
#pragma unroll
      for (int a = 0; a < 4; ++a) {
        r[a] = f32x4{0, 0, 0, 0};
#pragma unroll
        for (int e = 0; e < 4; ++e) r[a] = mfma16(sel, gl[a][e], r[a]);
        chk += r[a][0];
      }
      if (!__any(chk != chk) || s >= SPIN_CAP) break;
      bool pend = false;
#pragma unroll
      for (int a = 0; a < 4; ++a)
#pragma unroll
        for (int e = 0; e < 4; ++e)
#pragma unroll
          for (int i = 0; i < 4; ++i) pend |= gl[a][e][i] == POISON;
      if (!__any(pend)) break;
#pragma unroll
      for (int a = 0; a < 4; ++a)
#pragma unroll
        for (int e = 0; e < 4; ++e) gl[a][e] = gload16_sc1(goff[a], c.sWs, W_PART + (hq * 16 + 4 * e) * PSTR);
      asm volatile("s_waitcnt vmcnt(0)" : GL_ALL :: "memory");
    }
#pragma unroll
    for (int a = 0; a < 4; ++a) {
      const int G = w + 4 * a;
      const u32 ad = (g < 2 && G < 14) ? L_KV + (G * 16 + s16) * 16 + g * 8 : L_DUMP + lane * 8;
      lds_write8(ad, u32x2{pack2(r[a][0], r[a][1]), pack2(r[a][2], r[a][3])});
    }
  }
  STAMP(8);
  lds_bar();
  {
    const int qd = s32 >> 2, d0 = 4 * (qd >> 1) + (s32 & 3);
    const bool mine = (qd & 1) == hh;
    const u32 a0 = mine ? L_KV + (hh * 21 + d0) * 80 : (u32)L_ZERO;
    const u32 a1 = (mine && d0 < 5) ? L_KV + (hh * 21 + 16 + d0) * 80 : (u32)L_ZERO;
    f32x16 n0, n1;
#pragma unroll
    for (int i = 0; i < 16; ++i) { n0[i] = 0.f; n1[i] = 0.f; }
#pragma unroll
    for (int ks = 0; ks < 5; ++ks) {
      const u32x4 bf = {phq[4 * ks], phq[4 * ks + 1], phq[4 * ks + 2], phq[4 * ks + 3]};
      n1 = mfma32(lds_read16(a1 + ks * 16), bf, n1);
      n0 = mfma32(lds_read16(a0 + ks * 16), bf, n0);
    }
    const float rd = __builtin_amdgcn_rcpf(n1[4] + 1e-6f);
    u32 o[10];
#pragma unroll
    for (int i = 0; i < 8; ++i) o[i] = nopoison(pack2(n0[2 * i] * rd, n0[2 * i + 1] * rd));
#pragma unroll
    for (int i = 0; i < 2; ++i) o[8 + i] = nopoison(pack2(n1[2 * i] * rd, n1[2 * i + 1] * rd));
    const u32 so = W_O + ((rt * 8 + w * 2) * 80 + hq * 5) * 256;
    const u32 vb = (s32 >> 4) * (80 * 256) + s16 * 16;
    gstore16_sc1(u32x4{o[0], o[1], o[2], o[3]}, vb + hh * 768, c.sWs, so);
    gstore16_sc1(u32x4{o[4], o[5], o[6], o[7]}, vb + hh * 768 + 256, c.sWs, so);
    gstore8_sc1(u32x2{o[8], o[9]}, vb + 512 + hh * 8, c.sWs, so);
  }
  STAMP(9);
  wait_vm_bar<3>();
  STAMP(10);

  {
    const u32 so = W_O + (r64 * 4 + w) * (80 * 256);
    const u32 poff = lane < 16 ? (lane * 5 + 2) * 256 + 252 : OOB;
    for (int s = 0; s < SPIN_CAP; ++s) {
      u32 v = gload4_sc1(poff, c.sWs, so);
      asm volatile("s_waitcnt vmcnt(0)" : "+v"(v) :: "memory");
      if (!__any(v == POISON)) break;
    }
    STAMP(11);
    const u32 tb = (16 * (g >> 1) + 4 * (g & 1) + q) * 160 + p * 8;
    f32x4 d[5];
    for (int s = 0;; ++s) {
      u32x4 of[20];
#pragma unroll
      for (int ks = 0; ks < 20; ++ks) of[ks] = gload16_sc1(lane * 16, c.sWs, so + ks * 1024);
#pragma unroll
      for (int t = 0; t < 5; ++t) d[t] = f32x4{0, 0, 0, 0};
#pragma unroll
      for (int ks = 0; ks < 20; ++ks) {
        const u32 base = tb + (ks < 10 ? R1 + ks * 5120 : R0 + (ks - 10) * 5120);
        u32x4 a[5];
#pragma unroll
        for (int t = 0; t < 5; ++t) a[t] = tr_frag(base + t * 32, base + t * 32 + 1280);
        asm volatile("s_waitcnt vmcnt(%1)" : "+v"(of[ks]) : "n"(19 - ks));
#pragma unroll
        for (int t = 0; t < 5; ++t) d[t] = mfma16(a[t], of[ks], d[t]);
      }
      const float chk = d[0][0];
      if (!__any(chk != chk) || s >= SPIN_CAP) break;
      bool pend = false;
#pragma unroll
      for (int ks = 0; ks < 20; ++ks)
#pragma unroll
        for (int i = 0; i < 4; ++i) pend |= of[ks][i] == POISON;
      if (!__any(pend)) break;
    }
    STAMP(12);
    const u32 oo = (r64 * 64 + w * 16) * 1280 + ct * 160, vr = s16 * 1280;
#pragma unroll
    for (int t = 0; t < 4; t += 2) {
      u32 a0 = pack2(d[t][0], d[t][1]), a1 = pack2(d[t][2], d[t][3]), b0 = pack2(d[t + 1][0], d[t + 1][1]), b1 = pack2(d[t + 1][2], d[t + 1][3]);
      swap16(a0, b0); swap16(a1, b1);
      gstore16(u32x4{a0, a1, b0, b1}, vr + t * 32 + (g & 1) * 32 + (g >> 1) * 16, c.sOut, oo);
    }
    gstore8(u32x2{pack2(d[4][0], d[4][1]), pack2(d[4][2], d[4][3])}, vr + 128 + g * 8, c.sOut, oo);
    if (b == 0 && w == 0) gstore8_sc1(tagv, lane == 0 ? 0u : OOB, c.sWs, W_EPOCH);
  }
}

}

extern "C" void kernel_launch(void* const* d_in, const int* in_sizes, int n_in, void* d_out, int out_size, void* d_ws, size_t ws_size,
                              hipStream_t stream) {
  (void)in_sizes; (void)n_in; (void)out_size; (void)ws_size;
  hh::hedgehog_fused<<<256, 256, 0, stream>>>(d_in[0], d_in[1], d_in[2], d_in[3], d_out, d_ws);
}
```

```cpp
#include <hip/hip_runtime.h>
#include <hip/hip_bf16.h>
#include <stdint.h>

#define MIX 1

namespace hh {

typedef unsigned u32;
typedef unsigned long long u64;
typedef u32 u32x2 __attribute__((ext_vector_type(2)));
typedef u32 u32x4 __attribute__((ext_vector_type(4)));
typedef short s16x4 __attribute__((ext_vector_type(4)));
typedef __bf16 bf16x2 __attribute__((ext_vector_type(2)));
typedef __bf16 bf16x8 __attribute__((ext_vector_type(8)));
typedef float f32x2 __attribute__((ext_vector_type(2)));
typedef float f32x4 __attribute__((ext_vector_type(4)));
typedef float f32x16 __attribute__((ext_vector_type(16)));
#define LAS __attribute__((address_space(3)))
#define DI __device__ __forceinline__

constexpr int R0 = 0, R1 = 51200, R2 = 102400, R3 = 153600;
constexpr int L_DUMP = R3, L_WPH = R3 + 2048, L_ZERO = L_WPH + 3200, L_KV = R3 + 6144;
constexpr int L_PHI = R0, L_V = R0 + 20480;
constexpr int LDS_BYTES = 163840;

constexpr u32 W_EPOCH = 0, W_F1 = 4096, W_F2 = 8192, W_PART = 16384, PSTR = 3456;
constexpr u32 W_O = W_PART + 256 * PSTR;
constexpr u32 W_END = W_O + 128 * 80 * 256;
constexpr u32 W_STAMP = 8u << 20;
constexpr u32 OOB = 0x7ffffff0u;
constexpr u32 POISON = 0xffffffffu;
constexpr int SPIN_CAP = 1 << 18;

DI u32x4 make_srd(const void* p, u32 bytes) {
  const u64 a = (u64)p;
  return u32x4{(u32)a, (u32)(a >> 32) & 0xffffu, bytes, 0x00020000u};
}
DI void glds(u32 lds, u32 voff, u32x4 srd, u32 soff) {
  asm volatile("s_mov_b32 m0, %0\n\ts_nop 0\n\tbuffer_load_dwordx4 %1, %2, %3 offen lds"
               :: "s"(lds), "v"(voff), "s"(srd), "s"(soff) : "memory");
}
DI u32x4 gload16_sc1(u32 voff, u32x4 srd, u32 soff) {
  u32x4 r;
  asm volatile("buffer_load_dwordx4 %0, %1, %2, %3 offen sc1" : "=v"(r) : "v"(voff), "s"(srd), "s"(soff) : "memory");
  return r;
}
DI u32x2 gload8_sc1(u32 voff, u32x4 srd, u32 soff) {
  u32x2 r;
  asm volatile("buffer_load_dwordx2 %0, %1, %2, %3 offen sc1" : "=v"(r) : "v"(voff), "s"(srd), "s"(soff) : "memory");
  return r;
}
DI u32 gload4_sc1(u32 voff, u32x4 srd, u32 soff) {
  u32 r;
  asm volatile("buffer_load_dword %0, %1, %2, %3 offen sc1" : "=v"(r) : "v"(voff), "s"(srd), "s"(soff) : "memory");
  return r;
}
DI void gstore16(u32x4 d, u32 voff, u32x4 srd, u32 soff) {
  asm volatile("buffer_store_dwordx4 %0, %1, %2, %3 offen\n\ts_nop 1" :: "v"(d), "v"(voff), "s"(srd), "s"(soff) : "memory");
}
DI void gstore8(u32x2 d, u32 voff, u32x4 srd, u32 soff) {
  asm volatile("buffer_store_dwordx2 %0, %1, %2, %3 offen" :: "v"(d), "v"(voff), "s"(srd), "s"(soff) : "memory");
}
DI void gstore16_sc1(u32x4 d, u32 voff, u32x4 srd, u32 soff) {
  asm volatile("buffer_store_dwordx4 %0, %1, %2, %3 offen sc1\n\ts_nop 1" :: "v"(d), "v"(voff), "s"(srd), "s"(soff) : "memory");
}
DI void gstore8_sc1(u32x2 d, u32 voff, u32x4 srd, u32 soff) {
  asm volatile("buffer_store_dwordx2 %0, %1, %2, %3 offen sc1" :: "v"(d), "v"(voff), "s"(srd), "s"(soff) : "memory");
}
#define SCHED_FENCE() __builtin_amdgcn_sched_barrier(0)
template <int N> DI void wait_vm() { asm volatile("s_waitcnt vmcnt(%0)" :: "n"(N) : "memory"); }
template <int N> DI void wait_vm_bar() { asm volatile("s_waitcnt vmcnt(%0)\n\ts_barrier" :: "n"(N) : "memory"); }
template <int N> DI void wait_lgkm() { asm volatile("s_waitcnt lgkmcnt(%0)" :: "n"(N) : "memory"); }
DI void lds_bar() { asm volatile("s_waitcnt lgkmcnt(0)\n\ts_barrier" ::: "memory"); }
DI void mem_order() { asm volatile("" ::: "memory"); }

DI u32x2 tr_read(u32 addr) {
  return __builtin_bit_cast(u32x2, __builtin_amdgcn_ds_read_tr16_b64_v4i16((LAS s16x4*)(size_t)addr));
}
DI u32x4 tr_frag(u32 a0, u32 a1) {
  const u32x2 lo = tr_read(a0), hi = tr_read(a1);
  return u32x4{lo[0], lo[1], hi[0], hi[1]};
}
DI u32x4 lds_read16(u32 addr) { return *(LAS u32x4*)(size_t)addr; }
DI void lds_write16(u32 addr, u32x4 v) { *(LAS u32x4*)(size_t)addr = v; }
DI void lds_write8(u32 addr, u32x2 v) { *(LAS u32x2*)(size_t)addr = v; }
DI f32x4 mfma16(u32x4 a, u32x4 b, f32x4 c) {
  return __builtin_amdgcn_mfma_f32_16x16x32_bf16(__builtin_bit_cast(bf16x8, a), __builtin_bit_cast(bf16x8, b), c, 0, 0, 0);
}
DI f32x16 mfma32(u32x4 a, u32x4 b, f32x16 c) {
  return __builtin_amdgcn_mfma_f32_32x32x16_bf16(__builtin_bit_cast(bf16x8, a), __builtin_bit_cast(bf16x8, b), c, 0, 0, 0);
}
DI u32 pack2(float a, float b) { return __builtin_bit_cast(u32, __builtin_convertvector(f32x2{a, b}, bf16x2)); }
DI u32 nopoison(u32 x) { return x < POISON - 1 ? x : POISON - 1; }
DI void swap16(u32& a, u32& b) {
  const auto r = __builtin_amdgcn_permlane16_swap(a, b, false, false);
  a = r[0]; b = r[1];
}

#define STAMP(i) do {} while (0)

constexpr int r_need(int c) { return (5 * c + 4) / 4; }
constexpr int g_size(int c) { return c < 10 ? 4 + r_need(c) - (c ? r_need(c - 1) : -1) : (c == 10 ? 5 : 4); }

struct Ctx {
  u32x4 sX, sW, sWo, sWs, sOut;
  u32 vw[5];
  int w, rot;
};

template <int R> DI void issue_w(const Ctx& c, u32 region, u32 colbyte) {
  const int i = 4 * R + c.w;
  int kc = i / 5 + c.rot;
  kc = kc >= 10 ? kc - 10 : kc;
  u32 soff = (u32)kc * (64u * 3840u) + colbyte, lds = region + (u32)i * 1024u;
  if (R == 12) { soff = i >= 50 ? OOB : soff; lds = i >= 50 ? (u32)L_DUMP + (u32)(i - 50) * 1024u : lds; }
  glds(lds, c.vw[R % 5], c.sW, soff);
}
template <int R0_, int R1_> DI void issue_w_range(const Ctx& c, u32 region, u32 colbyte) {
  if constexpr (R0_ <= R1_) { issue_w<R0_>(c, region, colbyte); issue_w_range<R0_ + 1, R1_>(c, region, colbyte); }
}
template <int C> DI void issue_x(const Ctx& c, const u32 (&vx)[2], u32 rowbyte) {
  int kc = C + c.rot;
  kc = kc >= 10 ? kc - 10 : kc;
#pragma unroll
  for (int u = 0; u < 4; ++u)
    glds(R2 + (C % 3) * 16384 + c.w * 4096 + u * 1024, vx[u & 1], c.sX, rowbyte + (u32)kc * 128u + (u >> 1) * 20480u);
}

template <int KS> DI void gemm_step(const u32 (&ta)[3], const u32x4 (&xf)[20][2], f32x4 (&acc)[3][2]) {
#pragma unroll
  for (int t = 0; t < 3; ++t) {
    const u32x4 a = tr_frag(ta[t] + KS * 2560, ta[t] + KS * 2560 + 80);
    acc[t][0] = mfma16(a, xf[KS][0], acc[t][0]);
    acc[t][1] = mfma16(a, xf[KS][1], acc[t][1]);
  }
}

DI void acc_to_rows(const f32x4 (&acc)[3][2], u32 (&p)[10]) {
#pragma unroll
  for (int t = 0; t < 3; ++t)
#pragma unroll
    for (int i = 0; i < 2; ++i) {
      u32 a = pack2(acc[t][0][2 * i], acc[t][0][2 * i + 1]), b = pack2(acc[t][1][2 * i], acc[t][1][2 * i + 1]);
      swap16(a, b);
      p[4 * t + i] = a;
      if (t < 2) p[4 * t + 2 + i] = b;
    }
}

DI void load_wphi(u32 pairbase, int lane, u32x4 (&wf)[2][3]) {
  const int hh = lane >> 5, gi = (lane >> 4) & 1, q = (lane >> 2) & 3, p = lane & 3;
  const bool mine = (p & 1) == hh;
  const u32 row = pairbase + hh * 800 + q * 40;
#pragma unroll
  for (int mt = 0; mt < 2; ++mt) {
    const bool ok = mine && (mt == 0 || (gi == 0 && p < 2));
    const u32 col = mt == 0 ? (2 * gi + (p >> 1)) * 8 : 32;
    const u32 b = ok ? row + col + hh * 160 : (u32)L_ZERO;
    const u32 b4 = ok ? row + col + (hh ? 0 : 640) : (u32)L_ZERO;
    wf[mt][0] = tr_frag(b, b + 160);
    wf[mt][1] = tr_frag(b + 320, b + 480);
    const u32x2 l = tr_read(b4);
    wf[mt][2] = u32x4{l[0], l[1], 0u, 0u};
  }
}

DI void feature_map(const u32 (&p)[10], const u32x4 (&wf)[2][3], u32 (&phi)[20]) {
  f32x16 z;
#pragma unroll
  for (int i = 0; i < 16; ++i) z[i] = 0.f;
  const u32x4 b0 = {p[0], p[1], p[2], p[3]}, b1 = {p[4], p[5], p[6], p[7]}, b2 = {p[8], p[9], 0u, 0u};
  f32x16 d0 = mfma32(wf[0][0], b0, z), d1 = mfma32(wf[1][0], b0, z);
  d0 = mfma32(wf[0][1], b1, d0); d1 = mfma32(wf[1][1], b1, d1);
  d0 = mfma32(wf[0][2], b2, d0); d1 = mfma32(wf[1][2], b2, d1);
  float v[20];
#pragma unroll
  for (int i = 0; i < 16; ++i) v[i] = d0[i];
#pragma unroll
  for (int i = 0; i < 4; ++i) v[16 + i] = d1[i];
  float mx = fmaxf(v[0], v[1]), mn = fminf(v[0], v[1]);
#pragma unroll
  for (int i = 2; i < 20; i += 2) { mx = fmaxf(fmaxf(mx, v[i]), v[i + 1]); mn = fminf(fminf(mn, v[i]), v[i + 1]); }
  constexpr float L2E = 1.4426950408889634f;
  const float cx = -mx * L2E, cn = mn * L2E;
  f32x2 ep[10], en[10];
#pragma unroll
  for (int i = 0; i < 10; ++i) {
    const f32x2 x = {v[2 * i], v[2 * i + 1]};
    const f32x2 a = __builtin_elementwise_fma(x, f32x2{L2E, L2E}, f32x2{cx, cx});
    const f32x2 b = __builtin_elementwise_fma(x, f32x2{-L2E, -L2E}, f32x2{cn, cn});
    ep[i] = f32x2{__builtin_amdgcn_exp2f(a[0]), __builtin_amdgcn_exp2f(a[1])};
    en[i] = f32x2{__builtin_amdgcn_exp2f(b[0]), __builtin_amdgcn_exp2f(b[1])};
  }
  f32x2 sp = ep[0], sn = en[0];
#pragma unroll
  for (int i = 1; i < 10; ++i) { sp += ep[i]; sn += en[i]; }
  const float rp = __builtin_amdgcn_rcpf(sp[0] + sp[1]), rn = __builtin_amdgcn_rcpf(sn[0] + sn[1]);
#pragma unroll
  for (int i = 0; i < 10; ++i) {
    const f32x2 a = ep[i] * f32x2{rp, rp}, b = en[i] * f32x2{rn, rn};
    phi[i] = pack2(a[0], a[1]);
    phi[10 + i] = pack2(b[0], b[1]);
  }
}

template <int C> DI void pass1_iter(const Ctx& c, const u32 (&vx)[2], const u32 (&xa)[2], const u32 (&ta)[3], u32 rowbyte, u32 colk, u32 colv,
                                    u32x4 (&xf)[20][2], f32x4 (&acc)[3][2]) {
  wait_vm_bar<g_size(C + 1) + g_size(C + 2)>();
#pragma unroll
  for (int kk = 0; kk < 2; ++kk)
#pragma unroll
    for (int st = 0; st < 2; ++st) xf[2 * C + kk][st] = lds_read16(xa[kk] + (C % 3) * 16384 + st * 2048);
  wait_lgkm<0>();
  if constexpr (C + 3 < 10) {
    issue_x<C + 3>(c, vx, rowbyte);
    issue_w_range<r_need(C + 2) + 1, r_need(C + 3)>(c, R0, colk);
  } else if constexpr (C == 7) issue_w_range<0, 4>(c, R1, colv);
  else if constexpr (C == 8) issue_w_range<5, 8>(c, R1, colv);
  else issue_w_range<9, 12>(c, R1, colv);
  gemm_step<2 * C>(ta, xf, acc);
  gemm_step<2 * C + 1>(ta, xf, acc);
  SCHED_FENCE();
}
template <int C> DI void pass1_range(const Ctx& c, const u32 (&vx)[2], const u32 (&xa)[2], const u32 (&ta)[3], u32 rowbyte, u32 colk, u32 colv,
                                     u32x4 (&xf)[20][2], f32x4 (&acc)[3][2]) {
  if constexpr (C < 10) { pass1_iter<C>(c, vx, xa, ta, rowbyte, colk, colv, xf, acc); pass1_range<C + 1>(c, vx, xa, ta, rowbyte, colk, colv, xf, acc); }
}
template <int C, int AFTER> DI void pass_range(const u32 (&ta)[3], const u32x4 (&xf)[20][2], f32x4 (&acc)[3][2]) {
  if constexpr (C < 10) {
    wait_vm_bar<AFTER + 12 - r_need(C)>();
    gemm_step<2 * C>(ta, xf, acc);
    gemm_step<2 * C + 1>(ta, xf, acc);
    SCHED_FENCE();
    pass_range<C + 1, AFTER>(ta, xf, acc);
  }
}
template <int R> DI void issue_wo(const Ctx& c, const u32 (&vo)[5], u32 colbyte) {
  const int i = 4 * R + c.w;
  const u32 lds = i < 50 ? R1 + i * 1024 : R0 + (i - 50) * 1024;
  glds(lds, vo[R % 5], c.sWo, (u32)(i / 5) * (32u * 1280u) + colbyte);
}
template <int A, int B> DI void issue_wo_range(const Ctx& c, const u32 (&vo)[5], u32 colbyte) {
  if constexpr (A <= B) { issue_wo<A>(c, vo, colbyte); issue_wo_range<A + 1, B>(c, vo, colbyte); }
}

__global__ __launch_bounds__(256, 1) void hedgehog_fused(const void* __restrict__ x, const void* __restrict__ wqkv, const void* __restrict__ wphi,
                                                         const void* __restrict__ wo, void* __restrict__ out, void* __restrict__ ws) {
  STAMP(0);
  __shared__ __attribute__((aligned(16))) char smem[LDS_BYTES];
  asm volatile("" :: "v"((u32)(size_t)(LAS char*)smem));
  const int lane = threadIdx.x & 63;
  const int w = __builtin_amdgcn_readfirstlane(threadIdx.x >> 6);
  const int b = blockIdx.x, xg = b & 7, j = b >> 3;
  const int rt = (xg >> 1) * 4 + (j >> 3), hp = (xg & 1) * 8 + (j & 7), hq = hp ^ MIX;
  const int r64 = xg * 4 + (j >> 3), ct = j & 7;
  Ctx c;
  c.sX = make_srd(x, 2048 * 1280); c.sW = make_srd(wqkv, 640 * 3840); c.sWo = make_srd(wo, 640 * 1280);
  c.sWs = make_srd(ws, W_END); c.sOut = make_srd(out, 2048 * 1280);
  const u32x4 sPhi = make_srd(wphi, 25600);
  c.w = w; c.rot = (j + 3 * xg) % 10;
  const int g = lane >> 4, q = (lane >> 2) & 3, p = lane & 3, s16 = lane & 15, hh = lane >> 5, s32 = lane & 31;

  {
    u64 pc; asm volatile("s_getpc_b64 %0" : "=s"(pc));
    const u32x4 sC = make_srd((const void*)(pc & ~127ull), 27904);
    u32 dummy;
    asm volatile("buffer_load_dword %0, %1, %2, 0 offen\n\ts_waitcnt vmcnt(0)" : "=v"(dummy) : "v"((u32)threadIdx.x * 128u), "s"(sC) : "memory");
  }
  u32 vx[2];
#pragma unroll
  for (int u = 0; u < 2; ++u) { const int r = u * 8 + (lane >> 3); vx[u] = (w * 32 + r) * 1280 + (((lane & 7) ^ (r >> 1)) << 4); }
#pragma unroll
  for (int i = 0; i < 5; ++i) {
    const int cc = ((4 * i + w) % 5) * 64 + lane, rho = cc / 5, pc = cc - rho * 5;
    const int k = (rho & 48) + ((rho >> 3) & 1) * 8 + (rho & 1) * 4 + ((rho >> 1) & 3);
    c.vw[i] = k * 3840 + pc * 16;
  }
  const u32 rowbyte = rt * (128 * 1280), colq = hq * 80, colk = 1280 + hp * 80, colv = 2560 + hp * 80;

  u32x2 ep = gload8_sc1(0u, c.sWs, W_EPOCH);
  { const int cc = w * 64 + lane; glds(L_WPH + w * 1024, cc < 200 ? cc * 16 : OOB, sPhi, (hp >> 1) * 3200); }
  {
    const u32x4 ps = {POISON, POISON, POISON, POISON};
    const int cc = w * 64 + lane;
    gstore16_sc1(ps, cc < 210 ? cc * 16 : OOB, c.sWs, W_PART + (hp * 16 + rt) * PSTR);
#pragma unroll
    for (int i = 0; i < 3; ++i) {
      const int e = i * 64 + lane, st = e >= 80, r = e - st * 80;
      gstore16_sc1(ps, e < 160 ? st * (80 * 256) + r * 16 : OOB, c.sWs, W_O + ((rt * 8 + w * 2) * 80 + hq * 5) * 256);
    }
  }
  issue_x<0>(c, vx, rowbyte); issue_w_range<0, 1>(c, R0, colk);
  issue_x<1>(c, vx, rowbyte); issue_w_range<2, 2>(c, R0, colk);
  issue_x<2>(c, vx, rowbyte); issue_w_range<3, 3>(c, R0, colk);

  u32 xa[2], ta[3];
  xa[0] = R2 + w * 4096 + s16 * 128 + ((g ^ (s16 >> 1)) << 4);
  xa[1] = xa[0] ^ 64;
  const u32 trow = 16 * (g >> 1) + 8 * (g & 1) + 2 * q;
  ta[0] = R0 + trow * 80 + (p < 2 ? p : p + 4) * 8;
  ta[1] = ta[0] + 16;
  ta[2] = R0 + trow * 80 + (4 + (p >> 1)) * 8;

  asm volatile("s_waitcnt vmcnt(%1)\n\ts_barrier" : "+v"(ep) : "n"(g_size(0) + g_size(1) + g_size(2)) : "memory");
  const u64 epoch = ((u64)(u32)__builtin_amdgcn_readfirstlane(ep[1]) << 32) | (u32)__builtin_amdgcn_readfirstlane(ep[0]);
  const u64 tag = epoch * 6364136223846793005ull + 1442695040888963407ull;
  const u32x2 tagv = {(u32)tag, (u32)(tag >> 32)};
  u32x4 wf[2][3];
  load_wphi(L_WPH + (hp & 1) * 1600, lane, wf);
  STAMP(1);

  u32x4 xf[20][2];
  f32x4 acc[3][2];
#pragma unroll
  for (int t = 0; t < 3; ++t) { acc[t][0] = f32x4{0, 0, 0, 0}; acc[t][1] = f32x4{0, 0, 0, 0}; }
  pass1_range<0>(c, vx, xa, ta, rowbyte, colk, colv, xf, acc);
  STAMP(2);
  lds_bar();
  issue_w_range<0, 12>(c, R2, colq);
  gstore8_sc1(tagv, (w == 0 && lane < 2) ? (lane ? W_F2 + (rt * 16 + hq) * 8 : W_F1 + (hp * 16 + rt) * 8) : OOB, c.sWs, 0u);

  {
    u32 pk[10], phi[20];
    acc_to_rows(acc, pk);
    feature_map(pk, wf, phi);
    const u32 a = L_PHI + (hh * 128 + w * 32 + s32) * 80;
#pragma unroll
    for (int i = 0; i < 5; ++i) lds_write16(a + i * 16, u32x4{phi[4 * i], phi[4 * i + 1], phi[4 * i + 2], phi[4 * i + 3]});
  }
  STAMP(3);
#pragma unroll
  for (int t = 0; t < 3; ++t) { acc[t][0] = f32x4{0, 0, 0, 0}; acc[t][1] = f32x4{0, 0, 0, 0}; ta[t] += R1 - R0; }
  pass_range<0, 14>(ta, xf, acc);
  STAMP(4);
  {
    u32 pv[10];
    acc_to_rows(acc, pv);
    const u32 a = L_V + (hh * 128 + w * 32 + s32) * 48;
    lds_write16(a, u32x4{pv[0], pv[1], pv[2], pv[3]});
    lds_write16(a + 16, u32x4{pv[4], pv[5], pv[6], pv[7]});
    lds_write16(a + 32, u32x4{pv[8], pv[9], 0x3f80u, 0u});
  }
  lds_bar();
  {
    const int head = w >> 1, nt = w & 1;
    const u32 pa = L_PHI + (head * 128 + trow) * 80 + p * 8, pa2 = L_PHI + (head * 128 + trow) * 80 + (8 + (p & 1)) * 8;
    const u32 pb = L_V + (head * 128 + trow) * 48 + (nt ? 4 + (p & 1) : p) * 8;
    f32x4 d[3] = {f32x4{0, 0, 0, 0}, f32x4{0, 0, 0, 0}, f32x4{0, 0, 0, 0}};
#pragma unroll
    for (int ks = 0; ks < 4; ++ks) {
      const u32x4 bf = tr_frag(pb + ks * 1536, pb + ks * 1536 + 48);
      d[0] = mfma16(tr_frag(pa + ks * 2560, pa + ks * 2560 + 80), bf, d[0]);
      d[1] = mfma16(tr_frag(pa + ks * 2560 + 32, pa + ks * 2560 + 112), bf, d[1]);
      d[2] = mfma16(tr_frag(pa2 + ks * 2560, pa2 + ks * 2560 + 80), bf, d[2]);
    }
    u32 a0 = pack2(d[0][0], d[0][1]), a1 = pack2(d[0][2], d[0][3]), b0 = pack2(d[1][0], d[1][1]), b1 = pack2(d[1][2], d[1][3]);
    swap16(a0, b0); swap16(a1, b1);
    const bool ok = nt == 0 || s16 < 5;
    const u32 rowoff = (head * 21 + nt * 16 + s16) * 80;
    const u32 so = W_PART + (hp * 16 + rt) * PSTR;
    gstore16_sc1(u32x4{nopoison(a0), nopoison(a1), nopoison(b0), nopoison(b1)}, ok ? rowoff + (g & 1) * 32 + (g >> 1) * 16 : OOB, c.sWs, so);
    gstore8_sc1(u32x2{nopoison(pack2(d[2][0], d[2][1])), nopoison(pack2(d[2][2], d[2][3]))}, (ok && g < 2) ? rowoff + 64 + g * 8 : OOB, c.sWs, so);
  }
  STAMP(5);
  u32 vo[5];
#pragma unroll
  for (int i = 0; i < 5; ++i) {
    const int cc = ((4 * i + w) % 5) * 64 + lane, rho = cc / 10, pc = cc - rho * 10;
    const int k = (rho & 16) + ((rho >> 2) & 1) * 8 + ((rho >> 3) & 1) * 4 + (rho & 3);
    vo[i] = k * 1280 + pc * 16;
  }
  const u32 flagoff = lane < 16 ? W_F1 + (hq * 16 + lane) * 8 : (lane < 32 ? W_F2 + ((r64 >> 1) * 16 + lane - 16) * 8 : OOB);
  u32x2 fl = gload8_sc1(flagoff, c.sWs, 0u);
  issue_wo_range<0, 11>(c, vo, ct * 160);
#pragma unroll
  for (int t = 0; t < 3; ++t) { acc[t][0] = f32x4{0, 0, 0, 0}; acc[t][1] = f32x4{0, 0, 0, 0}; ta[t] += R2 - R1; }
  pass_range<0, 16>(ta, xf, acc);
  STAMP(6);

  asm volatile("s_waitcnt vmcnt(12)" : "+v"(fl) :: "memory");
  for (int s = 0; s < SPIN_CAP; ++s) {
    if (__all(lane >= 32 || (fl[0] == tagv[0] && fl[1] == tagv[1]))) break;
    fl = gload8_sc1(flagoff, c.sWs, 0u);
    asm volatile("s_waitcnt vmcnt(0)" : "+v"(fl) :: "memory");
  }
  u32 goff[4];
#pragma unroll
  for (int a = 0; a < 4; ++a) { const int ch = (w + 4 * a) * 16 + s16; goff[a] = ch < 210 ? g * PSTR + ch * 16 : OOB; }
  u32x4 gl[4][4];
#pragma unroll
  for (int a = 0; a < 4; ++a)
#pragma unroll
    for (int e = 0; e < 4; ++e) gl[a][e] = gload16_sc1(goff[a], c.sWs, W_PART + (hq * 16 + 4 * e) * PSTR);
  issue_wo_range<12, 24>(c, vo, ct * 160);

  u32 phq[20];
  {
    if (MIX) load_wphi(L_WPH + (hq & 1) * 1600, lane, wf);
    u32 pq[10];
    acc_to_rows(acc, pq);
    feature_map(pq, wf, phq);
  }
  SCHED_FENCE();
  STAMP(7);
#define GL_ALL "+v"(gl[0][0]), "+v"(gl[0][1]), "+v"(gl[0][2]), "+v"(gl[0][3]), "+v"(gl[1][0]), "+v"(gl[1][1]), "+v"(gl[1][2]), "+v"(gl[1][3]), \
               "+v"(gl[2][0]), "+v"(gl[2][1]), "+v"(gl[2][2]), "+v"(gl[2][3]), "+v"(gl[3][0]), "+v"(gl[3][1]), "+v"(gl[3][2]), "+v"(gl[3][3])
  asm volatile("s_waitcnt vmcnt(13)" : GL_ALL :: "memory");
  {
    const u32 one = 0x3f80u << ((s16 & 1) * 16);
    const u32x4 sel = {(s16 >> 1) == 0 ? one : 0u, (s16 >> 1) == 1 ? one : 0u, (s16 >> 1) == 2 ? one : 0u, (s16 >> 1) == 3 ? one : 0u};
    f32x4 r[4];
    for (int s = 0;; ++s) {
      float chk = 0.f;
#pragma unroll
      for (int a = 0; a < 4; ++a) {
        r[a] = f32x4{0, 0, 0, 0};
#pragma unroll
        for (int e = 0; e < 4; ++e) r[a] = mfma16(sel, gl[a][e], r[a]);
        chk += r[a][0];
      }
      if (!__any(chk != chk) || s >= SPIN_CAP) break;
      bool pend = false;
#pragma unroll
      for (int a = 0; a < 4; ++a)
#pragma unroll
        for (int e = 0; e < 4; ++e)
#pragma unroll
          for (int i = 0; i < 4; ++i) pend |= gl[a][e][i] == POISON;
      if (!__any(pend)) break;
#pragma unroll
      for (int a = 0; a < 4; ++a)
#pragma unroll
        for (int e = 0; e < 4; ++e) gl[a][e] = gload16_sc1(goff[a], c.sWs, W_PART + (hq * 16 + 4 * e) * PSTR);
      asm volatile("s_waitcnt vmcnt(0)" : GL_ALL :: "memory");
    }
#pragma unroll
    for (int a = 0; a < 4; ++a) {
      const int G = w + 4 * a;
      const u32 ad = (g < 2 && G < 14) ? L_KV + (G * 16 + s16) * 16 + g * 8 : L_DUMP + lane * 8;
      lds_write8(ad, u32x2{pack2(r[a][0], r[a][1]), pack2(r[a][2], r[a][3])});
    }
  }
  STAMP(8);
  lds_bar();
  {
    const int qd = s32 >> 2, d0 = 4 * (qd >> 1) + (s32 & 3);
    const bool mine = (qd & 1) == hh;
    const u32 a0 = mine ? L_KV + (hh * 21 + d0) * 80 : (u32)L_ZERO;
    const u32 a1 = (mine && d0 < 5) ? L_KV + (hh * 21 + 16 + d0) * 80 : (u32)L_ZERO;
    f32x16 n0, n1;
#pragma unroll
    for (int i = 0; i < 16; ++i) { n0[i] = 0.f; n1[i] = 0.f; }
#pragma unroll
    for (int ks = 0; ks < 5; ++ks) {
      const u32x4 bf = {phq[4 * ks], phq[4 * ks + 1], phq[4 * ks + 2], phq[4 * ks + 3]};
      n1 = mfma32(lds_read16(a1 + ks * 16), bf, n1);
      n0 = mfma32(lds_read16(a0 + ks * 16), bf, n0);
    }
    const float rd = __builtin_amdgcn_rcpf(n1[4] + 1e-6f);
    u32 o[10];
#pragma unroll
    for (int i = 0; i < 8; ++i) o[i] = nopoison(pack2(n0[2 * i] * rd, n0[2 * i + 1] * rd));
#pragma unroll
    for (int i = 0; i < 2; ++i) o[8 + i] = nopoison(pack2(n1[2 * i] * rd, n1[2 * i + 1] * rd));
    const u32 so = W_O + ((rt * 8 + w * 2) * 80 + hq * 5) * 256;
    const u32 vb = (s32 >> 4) * (80 * 256) + s16 * 16;
    gstore16_sc1(u32x4{o[0], o[1], o[2], o[3]}, vb + hh * 768, c.sWs, so);
    gstore16_sc1(u32x4{o[4], o[5], o[6], o[7]}, vb + hh * 768 + 256, c.sWs, so);
    gstore8_sc1(u32x2{o[8], o[9]}, vb + 512 + hh * 8, c.sWs, so);
  }
  STAMP(9);
  wait_vm_bar<3>();
  STAMP(10);

  {
    const u32 so = W_O + (r64 * 4 + w) * (80 * 256);
    const u32 poff = lane < 16 ? (lane * 5 + 2) * 256 + 252 : OOB;
    for (int s = 0; s < SPIN_CAP; ++s) {
      u32 v = gload4_sc1(poff, c.sWs, so);
      asm volatile("s_waitcnt vmcnt(0)" : "+v"(v) :: "memory");
      if (!__any(v == POISON)) break;
    }
    STAMP(11);
    const u32 tb = (16 * (g >> 1) + 4 * (g & 1) + q) * 160 + p * 8;
    f32x4 d[5];
    for (int s = 0;; ++s) {
      u32x4 of[20];
#pragma unroll
      for (int ks = 0; ks < 20; ++ks) of[ks] = gload16_sc1(lane * 16, c.sWs, so + ks * 1024);
#pragma unroll
      for (int t = 0; t < 5; ++t) d[t] = f32x4{0, 0, 0, 0};
#pragma unroll
      for (int ks = 0; ks < 20; ++ks) {
        const u32 base = tb + (ks < 10 ? R1 + ks * 5120 : R0 + (ks - 10) * 5120);
        u32x4 a[5];
#pragma unroll
        for (int t = 0; t < 5; ++t) a[t] = tr_frag(base + t * 32, base + t * 32 + 1280);
        asm volatile("s_waitcnt vmcnt(%1)" : "+v"(of[ks]) : "n"(19 - ks));
#pragma unroll
        for (int t = 0; t < 5; ++t) d[t] = mfma16(a[t], of[ks], d[t]);
      }
      const float chk = d[0][0];
      if (!__any(chk != chk) || s >= SPIN_CAP) break;
      bool pend = false;
#pragma unroll
      for (int ks = 0; ks < 20; ++ks)
#pragma unroll
        for (int i = 0; i < 4; ++i) pend |= of[ks][i] == POISON;
      if (!__any(pend)) break;
    }
    STAMP(12);
    const u32 oo = (r64 * 64 + w * 16) * 1280 + ct * 160, vr = s16 * 1280;
#pragma unroll
    for (int t = 0; t < 4; t += 2) {
      u32 a0 = pack2(d[t][0], d[t][1]), a1 = pack2(d[t][2], d[t][3]), b0 = pack2(d[t + 1][0], d[t + 1][1]), b1 = pack2(d[t + 1][2], d[t + 1][3]);
      swap16(a0, b0); swap16(a1, b1);
      gstore16(u32x4{a0, a1, b0, b1}, vr + t * 32 + (g & 1) * 32 + (g >> 1) * 16, c.sOut, oo);
    }
    gstore8(u32x2{pack2(d[4][0], d[4][1]), pack2(d[4][2], d[4][3])}, vr + 128 + g * 8, c.sOut, oo);
    if (b == 0 && w == 0) gstore8_sc1(tagv, lane == 0 ? 0u : OOB, c.sWs, W_EPOCH);
  }
}

}

extern "C" void kernel_launch(void* const* d_in, const int* in_sizes, int n_in, void* d_out, int out_size, void* d_ws, size_t ws_size,
                              hipStream_t stream) {
  (void)in_sizes; (void)n_in; (void)out_size; (void)ws_size;
  hh::hedgehog_fused<<<256, 256, 0, stream>>>(d_in[0], d_in[1], d_in[2], d_in[3], d_out, d_ws);
}
```

```cpp
#include <hip/hip_runtime.h>
#include <hip/hip_bf16.h>
#include <stdint.h>

#define MIX 1

namespace hh {

typedef unsigned u32;
typedef unsigned long long u64;
typedef u32 u32x2 __attribute__((ext_vector_type(2)));
typedef u32 u32x4 __attribute__((ext_vector_type(4)));
typedef short s16x4 __attribute__((ext_vector_type(4)));
typedef __bf16 bf16x2 __attribute__((ext_vector_type(2)));
typedef __bf16 bf16x8 __attribute__((ext_vector_type(8)));
typedef float f32x2 __attribute__((ext_vector_type(2)));
typedef float f32x4 __attribute__((ext_vector_type(4)));
typedef float f32x16 __attribute__((ext_vector_type(16)));
#define LAS __attribute__((address_space(3)))
#define DI __device__ __forceinline__

constexpr int R0 = 0, R1 = 51200, R2 = 102400, R3 = 153600;
constexpr int L_DUMP = R3, L_WPH = R3 + 2048, L_ZERO = L_WPH + 3200, L_KV = R3 + 6144;
constexpr int L_PHI = R0, L_V = R0 + 20480;
constexpr int LDS_BYTES = 163840;

constexpr u32 W_EPOCH = 0, W_F1 = 4096, W_F2 = 8192, W_PART = 16384, PSTR = 3456;
constexpr u32 W_O = W_PART + 256 * PSTR;
constexpr u32 W_END = W_O + 128 * 80 * 256;
constexpr u32 W_STAMP = 8u << 20;
constexpr u32 OOB = 0x7ffffff0u;
constexpr u32 POISON = 0xffffffffu;
constexpr int SPIN_CAP = 1 << 18;

DI u32x4 make_srd(const void* p, u32 bytes) {
  const u64 a = (u64)p;
  return u32x4{(u32)a, (u32)(a >> 32) & 0xffffu, bytes, 0x00020000u};
}
DI void glds(u32 lds, u32 voff, u32x4 srd, u32 soff) {
  asm volatile("s_mov_b32 m0, %0\n\ts_nop 0\n\tbuffer_load_dwordx4 %1, %2, %3 offen lds"
               :: "s"(lds), "v"(voff), "s"(srd), "s"(soff) : "memory");
}
DI u32x4 gload16_sc1(u32 voff, u32x4 srd, u32 soff) {
  u32x4 r;
  asm volatile("buffer_load_dwordx4 %0, %1, %2, %3 offen sc1" : "=v"(r) : "v"(voff), "s"(srd), "s"(soff) : "memory");
  return r;
}
DI u32x2 gload8_sc1(u32 voff, u32x4 srd, u32 soff) {
  u32x2 r;
  asm volatile("buffer_load_dwordx2 %0, %1, %2, %3 offen sc1" : "=v"(r) : "v"(voff), "s"(srd), "s"(soff) : "memory");
  return r;
}
DI u32 gload4_sc1(u32 voff, u32x4 srd, u32 soff) {
  u32 r;
  asm volatile("buffer_load_dword %0, %1, %2, %3 offen sc1" : "=v"(r) : "v"(voff), "s"(srd), "s"(soff) : "memory");
  return r;
}
DI void gstore16(u32x4 d, u32 voff, u32x4 srd, u32 soff) {
  asm volatile("buffer_store_dwordx4 %0, %1, %2, %3 offen\n\ts_nop 1" :: "v"(d), "v"(voff), "s"(srd), "s"(soff) : "memory");
}
DI void gstore8(u32x2 d, u32 voff, u32x4 srd, u32 soff) {
  asm volatile("buffer_store_dwordx2 %0, %1, %2, %3 offen" :: "v"(d), "v"(voff), "s"(srd), "s"(soff) : "memory");
}
DI void gstore16_sc1(u32x4 d, u32 voff, u32x4 srd, u32 soff) {
  asm volatile("buffer_store_dwordx4 %0, %1, %2, %3 offen sc1\n\ts_nop 1" :: "v"(d), "v"(voff), "s"(srd), "s"(soff) : "memory");
}
DI void gstore8_sc1(u32x2 d, u32 voff, u32x4 srd, u32 soff) {
  asm volatile("buffer_store_dwordx2 %0, %1, %2, %3 offen sc1" :: "v"(d), "v"(voff), "s"(srd), "s"(soff) : "memory");
}
#define SCHED_FENCE() __builtin_amdgcn_sched_barrier(0)
template <int N> DI void wait_vm() { asm volatile("s_waitcnt vmcnt(%0)" :: "n"(N) : "memory"); }
template <int N> DI void wait_vm_bar() { asm volatile("s_waitcnt vmcnt(%0)\n\ts_barrier" :: "n"(N) : "memory"); }
template <int N> DI void wait_lgkm() { asm volatile("s_waitcnt lgkmcnt(%0)" :: "n"(N) : "memory"); }
DI void lds_bar() { asm volatile("s_waitcnt lgkmcnt(0)\n\ts_barrier" ::: "memory"); }
DI void mem_order() { asm volatile("" ::: "memory"); }

DI u32x2 tr_read(u32 addr) {
  return __builtin_bit_cast(u32x2, __builtin_amdgcn_ds_read_tr16_b64_v4i16((LAS s16x4*)(size_t)addr));
}
DI u32x4 tr_frag(u32 a0, u32 a1) {
  const u32x2 lo = tr_read(a0), hi = tr_read(a1);
  return u32x4{lo[0], lo[1], hi[0], hi[1]};
}
DI u32x4 lds_read16(u32 addr) { return *(LAS u32x4*)(size_t)addr; }
DI void lds_write16(u32 addr, u32x4 v) { *(LAS u32x4*)(size_t)addr = v; }
DI void lds_write8(u32 addr, u32x2 v) { *(LAS u32x2*)(size_t)addr = v; }
DI f32x4 mfma16(u32x4 a, u32x4 b, f32x4 c) {
  return __builtin_amdgcn_mfma_f32_16x16x32_bf16(__builtin_bit_cast(bf16x8, a), __builtin_bit_cast(bf16x8, b), c, 0, 0, 0);
}
DI f32x16 mfma32(u32x4 a, u32x4 b, f32x16 c) {
  return __builtin_amdgcn_mfma_f32_32x32x16_bf16(__builtin_bit_cast(bf16x8, a), __builtin_bit_cast(bf16x8, b), c, 0, 0, 0);
}
DI u32 pack2(float a, float b) { return __builtin_bit_cast(u32, __builtin_convertvector(f32x2{a, b}, bf16x2)); }
DI u32 nopoison(u32 x) { return x < POISON - 1 ? x : POISON - 1; }
DI void swap16(u32& a, u32& b) {
  const auto r = __builtin_amdgcn_permlane16_swap(a, b, false, false);
  a = r[0]; b = r[1];
}

#define STAMP(i) do {} while (0)

constexpr int r_need(int c) { return (5 * c + 4) / 4; }
constexpr int g_size(int c) { return c < 10 ? 4 + r_need(c) - (c ? r_need(c - 1) : -1) : (c == 10 ? 5 : 4); }

struct Ctx {
  u32x4 sX, sW, sWo, sWs, sOut;
  u32 vw[5];
  int w, rot;
};

template <int R> DI void issue_w(const Ctx& c, u32 region, u32 colbyte) {
  const int i = 4 * R + c.w;
  int kc = i / 5 + c.rot;
  kc = kc >= 10 ? kc - 10 : kc;
  u32 soff = (u32)kc * (64u * 3840u) + colbyte, lds = region + (u32)i * 1024u;
  if (R == 12) { soff = i >= 50 ? OOB : soff; lds = i >= 50 ? (u32)L_DUMP + (u32)(i - 50) * 1024u : lds; }
  glds(lds, c.vw[R % 5], c.sW, soff);
}
template <int R0_, int R1_> DI void issue_w_range(const Ctx& c, u32 region, u32 colbyte) {
  if constexpr (R0_ <= R1_) { issue_w<R0_>(c, region, colbyte); issue_w_range<R0_ + 1, R1_>(c, region, colbyte); }
}
template <int C> DI void issue_x(const Ctx& c, const u32 (&vx)[2], u32 rowbyte) {
  int kc = C + c.rot;
  kc = kc >= 10 ? kc - 10 : kc;
#pragma unroll
  for (int u = 0; u < 4; ++u)
    glds(R2 + (C % 3) * 16384 + c.w * 4096 + u * 1024, vx[u & 1], c.sX, rowbyte + (u32)kc * 128u + (u >> 1) * 20480u);
}

template <int KS> DI void gemm_step(const u32 (&ta)[3], const u32x4 (&xf)[20][2], f32x4 (&acc)[3][2]) {
#pragma unroll
  for (int t = 0; t < 3; ++t) {
    const u32x4 a = tr_frag(ta[t] + KS * 2560, ta[t] + KS * 2560 + 80);
    acc[t][0] = mfma16(a, xf[KS][0], acc[t][0]);
    acc[t][1] = mfma16(a, xf[KS][1], acc[t][1]);
  }
}

DI void acc_to_rows(const f32x4 (&acc)[3][2], u32 (&p)[10]) {
#pragma unroll
  for (int t = 0; t < 3; ++t)
#pragma unroll
    for (int i = 0; i < 2; ++i) {
      u32 a = pack2(acc[t][0][2 * i], acc[t][0][2 * i + 1]), b = pack2(acc[t][1][2 * i], acc[t][1][2 * i + 1]);
      swap16(a, b);
      p[4 * t + i] = a;
      if (t < 2) p[4 * t + 2 + i] = b;
    }
}

DI void load_wphi(u32 pairbase, int lane, u32x4 (&wf)[2][3]) {
  const int hh = lane >> 5, gi = (lane >> 4) & 1, q = (lane >> 2) & 3, p = lane & 3;
  const bool mine = (p & 1) == hh;
  const u32 row = pairbase + hh * 800 + q * 40;
#pragma unroll
  for (int mt = 0; mt < 2; ++mt) {
    const bool ok = mine && (mt == 0 || (gi == 0 && p < 2));
    const u32 col = mt == 0 ? (2 * gi + (p >> 1)) * 8 : 32;
    const u32 b = ok ? row + col + hh * 160 : (u32)L_ZERO;
    const u32 b4 = ok ? row + col + (hh ? 0 : 640) : (u32)L_ZERO;
    wf[mt][0] = tr_frag(b, b + 160);
    wf[mt][1] = tr_frag(b + 320, b + 480);
    const u32x2 l = tr_read(b4);
    wf[mt][2] = u32x4{l[0], l[1], 0u, 0u};
  }
}

DI void feature_map(const u32 (&p)[10], const u32x4 (&wf)[2][3], u32 (&phi)[20]) {
  f32x16 z;
#pragma unroll
  for (int i = 0; i < 16; ++i) z[i] = 0.f;
  const u32x4 b0 = {p[0], p[1], p[2], p[3]}, b1 = {p[4], p[5], p[6], p[7]}, b2 = {p[8], p[9], 0u, 0u};
  f32x16 d0 = mfma32(wf[0][0], b0, z), d1 = mfma32(wf[1][0], b0, z);
  d0 = mfma32(wf[0][1], b1, d0); d1 = mfma32(wf[1][1], b1, d1);
  d0 = mfma32(wf[0][2], b2, d0); d1 = mfma32(wf[1][2], b2, d1);
  float v[20];
#pragma unroll
  for (int i = 0; i < 16; ++i) v[i] = d0[i];
#pragma unroll
  for (int i = 0; i < 4; ++i) v[16 + i] = d1[i];
  float mx = fmaxf(v[0], v[1]), mn = fminf(v[0], v[1]);
#pragma unroll
  for (int i = 2; i < 20; i += 2) { mx = fmaxf(fmaxf(mx, v[i]), v[i + 1]); mn = fminf(fminf(mn, v[i]), v[i + 1]); }
  constexpr float L2E = 1.4426950408889634f;
  const float cx = -mx * L2E, cn = mn * L2E;
  f32x2 ep[10], en[10];
#pragma unroll
  for (int i = 0; i < 10; ++i) {
    const f32x2 x = {v[2 * i], v[2 * i + 1]};
    const f32x2 a = __builtin_elementwise_fma(x, f32x2{L2E, L2E}, f32x2{cx, cx});
    const f32x2 b = __builtin_elementwise_fma(x, f32x2{-L2E, -L2E}, f32x2{cn, cn});
    ep[i] = f32x2{__builtin_amdgcn_exp2f(a[0]), __builtin_amdgcn_exp2f(a[1])};
    en[i] = f32x2{__builtin_amdgcn_exp2f(b[0]), __builtin_amdgcn_exp2f(b[1])};
  }
  f32x2 sp = ep[0], sn = en[0];
#pragma unroll
  for (int i = 1; i < 10; ++i) { sp += ep[i]; sn += en[i]; }
  const float rp = __builtin_amdgcn_rcpf(sp[0] + sp[1]), rn = __builtin_amdgcn_rcpf(sn[0] + sn[1]);
#pragma unroll
  for (int i = 0; i < 10; ++i) {
    const f32x2 a = ep[i] * f32x2{rp, rp}, b = en[i] * f32x2{rn, rn};
    phi[i] = pack2(a[0], a[1]);
    phi[10 + i] = pack2(b[0], b[1]);
  }
}

template <int C> DI void pass1_iter(const Ctx& c, const u32 (&vx)[2], const u32 (&xa)[2], const u32 (&ta)[3], u32 rowbyte, u32 colk, u32 colv,
                                    u32x4 (&xf)[20][2], f32x4 (&acc)[3][2]) {
  wait_vm_bar<g_size(C + 1) + g_size(C + 2)>();
#pragma unroll
  for (int kk = 0; kk < 2; ++kk)
#pragma unroll
    for (int st = 0; st < 2; ++st) xf[2 * C + kk][st] = lds_read16(xa[kk] + (C % 3) * 16384 + st * 2048);
  wait_lgkm<0>();
  if constexpr (C + 3 < 10) {
    issue_x<C + 3>(c, vx, rowbyte);
    issue_w_range<r_need(C + 2) + 1, r_need(C + 3)>(c, R0, colk);
  } else if constexpr (C == 7) issue_w_range<0, 4>(c, R1, colv);
  else if constexpr (C == 8) issue_w_range<5, 8>(c, R1, colv);
  else issue_w_range<9, 12>(c, R1, colv);
  gemm_step<2 * C>(ta, xf, acc);
  gemm_step<2 * C + 1>(ta, xf, acc);
  SCHED_FENCE();
}
template <int C> DI void pass1_range(const Ctx& c, const u32 (&vx)[2], const u32 (&xa)[2], const u32 (&ta)[3], u32 rowbyte, u32 colk, u32 colv,
                                     u32x4 (&xf)[20][2], f32x4 (&acc)[3][2]) {
  if constexpr (C < 10) { pass1_iter<C>(c, vx, xa, ta, rowbyte, colk, colv, xf, acc); pass1_range<C + 1>(c, vx, xa, ta, rowbyte, colk, colv, xf, acc); }
}
template <int C, int AFTER> DI void pass_range(const u32 (&ta)[3], const u32x4 (&xf)[20][2], f32x4 (&acc)[3][2]) {
  if constexpr (C < 10) {
    wait_vm_bar<AFTER + 12 - r_need(C)>();
    gemm_step<2 * C>(ta, xf, acc);
    gemm_step<2 * C + 1>(ta, xf, acc);
    SCHED_FENCE();
    pass_range<C + 1, AFTER>(ta, xf, acc);
  }
}
template <int R> DI void issue_wo(const Ctx& c, const u32 (&vo)[5], u32 colbyte) {
  const int i = 4 * R + c.w;
  const u32 lds = i < 50 ? R1 + i * 1024 : R0 + (i - 50) * 1024;
  glds(lds, vo[R % 5], c.sWo, (u32)(i / 5) * (32u * 1280u) + colbyte);
}
template <int A, int B> DI void issue_wo_range(const Ctx& c, const u32 (&vo)[5], u32 colbyte) {
  if constexpr (A <= B) { issue_wo<A>(c, vo, colbyte); issue_wo_range<A + 1, B>(c, vo, colbyte); }
}

__global__ __launch_bounds__(256, 1) void hedgehog_fused(const void* __restrict__ x, const void* __restrict__ wqkv, const void* __restrict__ wphi,
                                                         const void* __restrict__ wo, void* __restrict__ out, void* __restrict__ ws) {
  STAMP(0);
  __shared__ __attribute__((aligned(16))) char smem[LDS_BYTES];
  asm volatile("" :: "v"((u32)(size_t)(LAS char*)smem));
  const int lane = threadIdx.x & 63;
  const int w = __builtin_amdgcn_readfirstlane(threadIdx.x >> 6);
  const int b = blockIdx.x, xg = b & 7, j = b >> 3;
  const int rt = (xg >> 1) * 4 + (j >> 3), hp = (xg & 1) * 8 + (j & 7), hq = hp ^ MIX;
  const int r64 = xg * 4 + (j >> 3), ct = j & 7;
  Ctx c;
  c.sX = make_srd(x, 2048 * 1280); c.sW = make_srd(wqkv, 640 * 3840); c.sWo = make_srd(wo, 640 * 1280);
  c.sWs = make_srd(ws, W_END); c.sOut = make_srd(out, 2048 * 1280);
  const u32x4 sPhi = make_srd(wphi, 25600);
  c.w = w; c.rot = (j + 3 * xg) % 10;
  const int g = lane >> 4, q = (lane >> 2) & 3, p = lane & 3, s16 = lane & 15, hh = lane >> 5, s32 = lane & 31;

  u32 vx[2];
#pragma unroll
  for (int u = 0; u < 2; ++u) { const int r = u * 8 + (lane >> 3); vx[u] = (w * 32 + r) * 1280 + (((lane & 7) ^ (r >> 1)) << 4); }
#pragma unroll
  for (int i = 0; i < 5; ++i) {
    const int cc = ((4 * i + w) % 5) * 64 + lane, rho = cc / 5, pc = cc - rho * 5;
    const int k = (rho & 48) + ((rho >> 3) & 1) * 8 + (rho & 1) * 4 + ((rho >> 1) & 3);
    c.vw[i] = k * 3840 + pc * 16;
  }
  const u32 rowbyte = rt * (128 * 1280), colq = hq * 80, colk = 1280 + hp * 80, colv = 2560 + hp * 80;

  u32x2 ep = gload8_sc1(0u, c.sWs, W_EPOCH);
  { const int cc = w * 64 + lane; glds(L_WPH + w * 1024, cc < 200 ? cc * 16 : OOB, sPhi, (hp >> 1) * 3200); }
  {
    const u32x4 ps = {POISON, POISON, POISON, POISON};
    const int cc = w * 64 + lane;
    gstore16_sc1(ps, cc < 210 ? cc * 16 : OOB, c.sWs, W_PART + (hp * 16 + rt) * PSTR);
#pragma unroll
    for (int i = 0; i < 3; ++i) {
      const int e = i * 64 + lane, st = e >= 80, r = e - st * 80;
      gstore16_sc1(ps, e < 160 ? st * (80 * 256) + r * 16 : OOB, c.sWs, W_O + ((rt * 8 + w * 2) * 80 + hq * 5) * 256);
    }
  }
  issue_x<0>(c, vx, rowbyte); issue_w_range<0, 1>(c, R0, colk);
  issue_x<1>(c, vx, rowbyte); issue_w_range<2, 2>(c, R0, colk);
  issue_x<2>(c, vx, rowbyte); issue_w_range<3, 3>(c, R0, colk);

  u32 xa[2], ta[3];
  xa[0] = R2 + w * 4096 + s16 * 128 + ((g ^ (s16 >> 1)) << 4);
  xa[1] = xa[0] ^ 64;
  const u32 trow = 16 * (g >> 1) + 8 * (g & 1) + 2 * q;
  ta[0] = R0 + trow * 80 + (p < 2 ? p : p + 4) * 8;
  ta[1] = ta[0] + 16;
  ta[2] = R0 + trow * 80 + (4 + (p >> 1)) * 8;

  asm volatile("s_waitcnt vmcnt(%1)\n\ts_barrier" : "+v"(ep) : "n"(g_size(0) + g_size(1) + g_size(2)) : "memory");
  const u64 epoch = ((u64)(u32)__builtin_amdgcn_readfirstlane(ep[1]) << 32) | (u32)__builtin_amdgcn_readfirstlane(ep[0]);
  const u64 tag = epoch * 6364136223846793005ull + 1442695040888963407ull;
  const u32x2 tagv = {(u32)tag, (u32)(tag >> 32)};
  u32x4 wf[2][3];
  load_wphi(L_WPH + (hp & 1) * 1600, lane, wf);
  STAMP(1);

  u32x4 xf[20][2];
  f32x4 acc[3][2];
#pragma unroll
  for (int t = 0; t < 3; ++t) { acc[t][0] = f32x4{0, 0, 0, 0}; acc[t][1] = f32x4{0, 0, 0, 0}; }
  pass1_range<0>(c, vx, xa, ta, rowbyte, colk, colv, xf, acc);
  STAMP(2);
  lds_bar();
  issue_w_range<0, 12>(c, R2, colq);
  gstore8_sc1(tagv, (w == 0 && lane < 2) ? (lane ? W_F2 + (rt * 16 + hq) * 8 : W_F1 + (hp * 16 + rt) * 8) : OOB, c.sWs, 0u);

  {
    u32 pk[10], phi[20];
    acc_to_rows(acc, pk);
    feature_map(pk, wf, phi);
    const u32 a = L_PHI + (hh * 128 + w * 32 + s32) * 80;
#pragma unroll
    for (int i = 0; i < 5; ++i) lds_write16(a + i * 16, u32x4{phi[4 * i], phi[4 * i + 1], phi[4 * i + 2], phi[4 * i + 3]});
  }
  STAMP(3);
#pragma unroll
  for (int t = 0; t < 3; ++t) { acc[t][0] = f32x4{0, 0, 0, 0}; acc[t][1] = f32x4{0, 0, 0, 0}; ta[t] += R1 - R0; }
  pass_range<0, 14>(ta, xf, acc);
  STAMP(4);
  {
    u32 pv[10];
    acc_to_rows(acc, pv);
    const u32 a = L_V + (hh * 128 + w * 32 + s32) * 48;
    lds_write16(a, u32x4{pv[0], pv[1], pv[2], pv[3]});
    lds_write16(a + 16, u32x4{pv[4], pv[5], pv[6], pv[7]});
    lds_write16(a + 32, u32x4{pv[8], pv[9], 0x3f80u, 0u});
  }
  lds_bar();
  {
    const int head = w >> 1, nt = w & 1;
    const u32 pa = L_PHI + (head * 128 + trow) * 80 + p * 8, pa2 = L_PHI + (head * 128 + trow) * 80 + (8 + (p & 1)) * 8;
    const u32 pb = L_V + (head * 128 + trow) * 48 + (nt ? 4 + (p & 1) : p) * 8;
    f32x4 d[3] = {f32x4{0, 0, 0, 0}, f32x4{0, 0, 0, 0}, f32x4{0, 0, 0, 0}};
#pragma unroll
    for (int ks = 0; ks < 4; ++ks) {
      const u32x4 bf = tr_frag(pb + ks * 1536, pb + ks * 1536 + 48);
      d[0] = mfma16(tr_frag(pa + ks * 2560, pa + ks * 2560 + 80), bf, d[0]);
      d[1] = mfma16(tr_frag(pa + ks * 2560 + 32, pa + ks * 2560 + 112), bf, d[1]);
      d[2] = mfma16(tr_frag(pa2 + ks * 2560, pa2 + ks * 2560 + 80), bf, d[2]);
    }
    u32 a0 = pack2(d[0][0], d[0][1]), a1 = pack2(d[0][2], d[0][3]), b0 = pack2(d[1][0], d[1][1]), b1 = pack2(d[1][2], d[1][3]);
    swap16(a0, b0); swap16(a1, b1);
    const bool ok = nt == 0 || s16 < 5;
    const u32 rowoff = (head * 21 + nt * 16 + s16) * 80;
    const u32 so = W_PART + (hp * 16 + rt) * PSTR;
    gstore16_sc1(u32x4{nopoison(a0), nopoison(a1), nopoison(b0), nopoison(b1)}, ok ? rowoff + (g & 1) * 32 + (g >> 1) * 16 : OOB, c.sWs, so);
    gstore8_sc1(u32x2{nopoison(pack2(d[2][0], d[2][1])), nopoison(pack2(d[2][2], d[2][3]))}, (ok && g < 2) ? rowoff + 64 + g * 8 : OOB, c.sWs, so);
  }
  STAMP(5);
  u32 vo[5];
#pragma unroll
  for (int i = 0; i < 5; ++i) {
    const int cc = ((4 * i + w) % 5) * 64 + lane, rho = cc / 10, pc = cc - rho * 10;
    const int k = (rho & 16) + ((rho >> 2) & 1) * 8 + ((rho >> 3) & 1) * 4 + (rho & 3);
    vo[i] = k * 1280 + pc * 16;
  }
  const u32 flagoff = lane < 16 ? W_F1 + (hq * 16 + lane) * 8 : (lane < 32 ? W_F2 + ((r64 >> 1) * 16 + lane - 16) * 8 : OOB);
  u32x2 fl = gload8_sc1(flagoff, c.sWs, 0u);
  issue_wo_range<0, 11>(c, vo, ct * 160);
#pragma unroll
  for (int t = 0; t < 3; ++t) { acc[t][0] = f32x4{0, 0, 0, 0}; acc[t][1] = f32x4{0, 0, 0, 0}; ta[t] += R2 - R1; }
  pass_range<0, 16>(ta, xf, acc);
  STAMP(6);

  asm volatile("s_waitcnt vmcnt(12)" : "+v"(fl) :: "memory");
  for (int s = 0; s < SPIN_CAP; ++s) {
    if (__all(lane >= 32 || (fl[0] == tagv[0] && fl[1] == tagv[1]))) break;
    fl = gload8_sc1(flagoff, c.sWs, 0u);
    asm volatile("s_waitcnt vmcnt(0)" : "+v"(fl) :: "memory");
  }
  u32 goff[4];
#pragma unroll
  for (int a = 0; a < 4; ++a) { const int ch = (w + 4 * a) * 16 + s16; goff[a] = ch < 210 ? g * PSTR + ch * 16 : OOB; }
  u32x4 gl[4][4];
#pragma unroll
  for (int a = 0; a < 4; ++a)
#pragma unroll
    for (int e = 0; e < 4; ++e) gl[a][e] = gload16_sc1(goff[a], c.sWs, W_PART + (hq * 16 + 4 * e) * PSTR);
  issue_wo_range<12, 24>(c, vo, ct * 160);

  u32 phq[20];
  {
    if (MIX) load_wphi(L_WPH + (hq & 1) * 1600, lane, wf);
    u32 pq[10];
    acc_to_rows(acc, pq);
    feature_map(pq, wf, phq);
  }
  SCHED_FENCE();
  STAMP(7);
#define GL_ALL "+v"(gl[0][0]), "+v"(gl[0][1]), "+v"(gl[0][2]), "+v"(gl[0][3]), "+v"(gl[1][0]), "+v"(gl[1][1]), "+v"(gl[1][2]), "+v"(gl[1][3]), \
               "+v"(gl[2][0]), "+v"(gl[2][1]), "+v"(gl[2][2]), "+v"(gl[2][3]), "+v"(gl[3][0]), "+v"(gl[3][1]), "+v"(gl[3][2]), "+v"(gl[3][3])
  asm volatile("s_waitcnt vmcnt(13)" : GL_ALL :: "memory");
  {
    const u32 one = 0x3f80u << ((s16 & 1) * 16);
    const u32x4 sel = {(s16 >> 1) == 0 ? one : 0u, (s16 >> 1) == 1 ? one : 0u, (s16 >> 1) == 2 ? one : 0u, (s16 >> 1) == 3 ? one : 0u};
    f32x4 r[4];
    for (int s = 0;; ++s) {
      float chk = 0.f;
#pragma unroll
      for (int a = 0; a < 4; ++a) {
        r[a] = f32x4{0, 0, 0, 0};
#pragma unroll
        for (int e = 0; e < 4; ++e) r[a] = mfma16(sel, gl[a][e], r[a]);
        chk += r[a][0];
      }
      if (!__any(chk != chk) || s >= SPIN_CAP) break;
      bool pend = false;
#pragma unroll
      for (int a = 0; a < 4; ++a)
#pragma unroll
        for (int e = 0; e < 4; ++e)
#pragma unroll
          for (int i = 0; i < 4; ++i) pend |= gl[a][e][i] == POISON;
      if (!__any(pend)) break;
#pragma unroll
      for (int a = 0; a < 4; ++a)
#pragma unroll
        for (int e = 0; e < 4; ++e) gl[a][e] = gload16_sc1(goff[a], c.sWs, W_PART + (hq * 16 + 4 * e) * PSTR);
      asm volatile("s_waitcnt vmcnt(0)" : GL_ALL :: "memory");
    }
#pragma unroll
    for (int a = 0; a < 4; ++a) {
      const int G = w + 4 * a;
      const u32 ad = (g < 2 && G < 14) ? L_KV + (G * 16 + s16) * 16 + g * 8 : L_DUMP + lane * 8;
      lds_write8(ad, u32x2{pack2(r[a][0], r[a][1]), pack2(r[a][2], r[a][3])});
    }
  }
  STAMP(8);
  lds_bar();
  {
    const int qd = s32 >> 2, d0 = 4 * (qd >> 1) + (s32 & 3);
    const bool mine = (qd & 1) == hh;
    const u32 a0 = mine ? L_KV + (hh * 21 + d0) * 80 : (u32)L_ZERO;
    const u32 a1 = (mine && d0 < 5) ? L_KV + (hh * 21 + 16 + d0) * 80 : (u32)L_ZERO;
    f32x16 n0, n1;
#pragma unroll
    for (int i = 0; i < 16; ++i) { n0[i] = 0.f; n1[i] = 0.f; }
#pragma unroll
    for (int ks = 0; ks < 5; ++ks) {
      const u32x4 bf = {phq[4 * ks], phq[4 * ks + 1], phq[4 * ks + 2], phq[4 * ks + 3]};
      n1 = mfma32(lds_read16(a1 + ks * 16), bf, n1);
      n0 = mfma32(lds_read16(a0 + ks * 16), bf, n0);
    }
    const float rd = __builtin_amdgcn_rcpf(n1[4] + 1e-6f);
    u32 o[10];
#pragma unroll
    for (int i = 0; i < 8; ++i) o[i] = nopoison(pack2(n0[2 * i] * rd, n0[2 * i + 1] * rd));
#pragma unroll
    for (int i = 0; i < 2; ++i) o[8 + i] = nopoison(pack2(n1[2 * i] * rd, n1[2 * i + 1] * rd));
    const u32 so = W_O + ((rt * 8 + w * 2) * 80 + hq * 5) * 256;
    const u32 vb = (s32 >> 4) * (80 * 256) + s16 * 16;
    gstore16_sc1(u32x4{o[0], o[1], o[2], o[3]}, vb + hh * 768, c.sWs, so);
    gstore16_sc1(u32x4{o[4], o[5], o[6], o[7]}, vb + hh * 768 + 256, c.sWs, so);
    gstore8_sc1(u32x2{o[8], o[9]}, vb + 512 + hh * 8, c.sWs, so);
  }
  STAMP(9);
  wait_vm_bar<3>();
  STAMP(10);

  {
    const u32 so = W_O + (r64 * 4 + w) * (80 * 256);
    const u32 poff = lane < 16 ? (lane * 5 + 2) * 256 + 252 : OOB;
    for (int s = 0; s < SPIN_CAP; ++s) {
      u32 v = gload4_sc1(poff, c.sWs, so);
      asm volatile("s_waitcnt vmcnt(0)" : "+v"(v) :: "memory");
      if (!__any(v == POISON)) break;
    }
    STAMP(11);
    const u32 tb = (16 * (g >> 1) + 4 * (g & 1) + q) * 160 + p * 8;
    f32x4 d[5];
    for (int s = 0;; ++s) {
      u32x4 of[20];
#pragma unroll
      for (int ks = 0; ks < 20; ++ks) of[ks] = gload16_sc1(lane * 16, c.sWs, so + ks * 1024);
#pragma unroll
      for (int t = 0; t < 5; ++t) d[t] = f32x4{0, 0, 0, 0};
#pragma unroll
      for (int ks = 0; ks < 20; ++ks) {
        const u32 base = tb + (ks < 10 ? R1 + ks * 5120 : R0 + (ks - 10) * 5120);
        u32x4 a[5];
#pragma unroll
        for (int t = 0; t < 5; ++t) a[t] = tr_frag(base + t * 32, base + t * 32 + 1280);
        asm volatile("s_waitcnt vmcnt(%1)" : "+v"(of[ks]) : "n"(19 - ks));
#pragma unroll
        for (int t = 0; t < 5; ++t) d[t] = mfma16(a[t], of[ks], d[t]);
      }
      const float chk = d[0][0];
      if (!__any(chk != chk) || s >= SPIN_CAP) break;
      bool pend = false;
#pragma unroll
      for (int ks = 0; ks < 20; ++ks)
#pragma unroll
        for (int i = 0; i < 4; ++i) pend |= of[ks][i] == POISON;
      if (!__any(pend)) break;
    }
    STAMP(12);
    const u32 oo = (r64 * 64 + w * 16) * 1280 + ct * 160, vr = s16 * 1280;
#pragma unroll
    for (int t = 0; t < 4; t += 2) {
      u32 a0 = pack2(d[t][0], d[t][1]), a1 = pack2(d[t][2], d[t][3]), b0 = pack2(d[t + 1][0], d[t + 1][1]), b1 = pack2(d[t + 1][2], d[t + 1][3]);
      swap16(a0, b0); swap16(a1, b1);
      gstore16(u32x4{a0, a1, b0, b1}, vr + t * 32 + (g & 1) * 32 + (g >> 1) * 16, c.sOut, oo);
    }
    gstore8(u32x2{pack2(d[4][0], d[4][1]), pack2(d[4][2], d[4][3])}, vr + 128 + g * 8, c.sOut, oo);
    if (b == 0 && w == 0) gstore8_sc1(tagv, lane == 0 ? 0u : OOB, c.sWs, W_EPOCH);
  }
}

}

__global__ __launch_bounds__(256,1) void tiny(unsigned* p) { asm volatile("v_mov_b32 v250, 0\n\tv_accvgpr_write_b32 a30, 0" ::: "v250", "a30"); if (p == nullptr) p[0] = 1; }
extern "C" void kernel_launch(void* const* d_in, const int* in_sizes, int n_in, void* d_out, int out_size, void* d_ws, size_t ws_size,
                              hipStream_t stream) {
  (void)in_sizes; (void)n_in; (void)out_size; (void)ws_size;
  hh::hedgehog_fused<<<256, 256, 0, stream>>>(d_in[0], d_in[1], d_in[2], d_in[3], d_out, d_ws);
  tiny<<<256, 256, 0, stream>>>((unsigned*)d_ws);
}
```

```cpp
#include <hip/hip_runtime.h>
#include <hip/hip_bf16.h>

__global__ __launch_bounds__(512, 2) void hedgehog_fused(const void* x, const void* wqkv, const void* wphi, const void* wo, void* out, void* ws) {
  __shared__ __attribute__((aligned(16))) char smem[163840];
#if defined(__HIP_DEVICE_COMPILE__)
  asm volatile(
    "s_load_dwordx2 s[20:21], %0, 0x0\n"
    "s_load_dwordx2 s[32:33], %0, 0x8\n"
    "s_load_dwordx2 s[36:37], %0, 0x28\n"
    "s_load_dwordx2 s[40:41], %0, 0x10\n"
    "s_load_dwordx2 s[44:45], %0, 0x18\n"
    "s_load_dwordx2 s[48:49], %0, 0x20\n"
    "v_lshrrev_b32 v11, 6, %2\n"
    "v_and_b32 v1, 63, %2\n"
    "v_readfirstlane_b32 s17, v11\n"
    "s_mov_b32 s18, %1\n"
    "s_and_b32 s5, s18, 7\n"
    "s_lshr_b32 s6, s18, 3\n"
    "s_mul_i32 s12, s5, 3\n"
    "s_add_u32 s12, s12, s6\n"
    "s_mul_i32 s15, s12, 26\n"
    "s_lshr_b32 s15, s15, 8\n"
    "s_mul_i32 s15, s15, 10\n"
    "s_sub_u32 s12, s12, s15\n"
    "s_lshl_b32 s65, s12, 7\n"
    "s_and_b32 s4, s17, 3\n"
    "s_lshr_b32 s7, s5, 1\n"
    "s_lshl_b32 s7, s7, 2\n"
    "s_lshr_b32 s15, s6, 3\n"
    "s_add_u32 s7, s7, s15\n"
    "s_lshl_b32 s84, s4, 12\n"
    "s_and_b32 s11, s6, 7\n"
    "s_and_b32 s8, s5, 1\n"
    "s_lshl_b32 s8, s8, 3\n"
    "s_add_u32 s8, s8, s11\n"
    "s_cmp_lt_u32 s17, 4\n"
    "s_cbranch_scc1 L_A%=\n"
    "v_lshrrev_b32 v11, 3, v1\n"
    "v_lshrrev_b32 v12, 4, v1\n"
    "v_xor_b32 v12, v12, v1\n"
    "v_and_b32 v12, 7, v12\n"
    "v_mul_u32_u24 v11, 0x500, v11\n"
    "v_lshl_or_b32 v2, v12, 4, v11\n"
    "s_lshl_b32 s16, s7, 2\n"
    "v_xor_b32 v3, 64, v2\n"
    "s_add_u32 s16, s16, s4\n"
    "v_add_u32 v3, 9216, v3\n"
    "s_mul_i32 s14, s16, 40960\n"
    "v_add_u32 v4, 18432, v2\n"
    "v_add_u32 v5, 18432, v3\n"
    "s_mov_b32 s22, 2621440\n"
    "s_mov_b32 s23, 0x20000\n"
    "s_add_u32 s15, s65, s14\n"
    "s_add_u32 m0, s84, 102400\n"
    "s_waitcnt lgkmcnt(0)\n"
    "buffer_load_dwordx4 v2, s[20:23], s15 offen offset:0 lds\n"
    "buffer_load_dwordx4 v3, s[20:23], s15 offen offset:1024 lds\n"
    "buffer_load_dwordx4 v4, s[20:23], s15 offen offset:2048 lds\n"
    "buffer_load_dwordx4 v5, s[20:23], s15 offen offset:3072 lds\n"
    "s_add_u32 s16, s65, 128\n"
    "s_cmp_eq_u32 s16, 1280\n"
    "s_cselect_b32 s66, 0, s16\n"
    "s_add_u32 s15, s66, s14\n"
    "s_add_u32 m0, s84, 118784\n"
    "s_nop 0\n"
    "buffer_load_dwordx4 v2, s[20:23], s15 offen offset:0 lds\n"
    "buffer_load_dwordx4 v3, s[20:23], s15 offen offset:1024 lds\n"
    "buffer_load_dwordx4 v4, s[20:23], s15 offen offset:2048 lds\n"
    "buffer_load_dwordx4 v5, s[20:23], s15 offen offset:3072 lds\n"
    "s_branch L_B%=\n"
    "L_A%=:\n"
    "s_lshl_b32 s17, s4, 6\n"
    "v_or_b32 v11, s17, v1\n"
    "v_mul_u32_u24 v12, 0x3334, v11\n"
    "v_lshrrev_b32 v12, 16, v12\n"
    "v_mul_u32_u24 v13, 5, v12\n"
    "v_sub_u32 v11, v11, v13\n"
    "v_bfe_u32 v13, v12, 1, 2\n"
    "v_and_or_b32 v13, v12, 56, v13\n"
    "v_and_b32 v12, 1, v12\n"
    "v_lshl_or_b32 v13, v12, 2, v13\n"
    "v_mul_u32_u24 v13, 0xf00, v13\n"
    "v_lshl_add_u32 v32, v11, 4, v13\n"
    "s_lshl_b32 s13, s4, 10\n"
    "s_mul_i32 s15, s8, 80\n"
    "s_add_u32 s15, s15, 1280\n"
    "s_sub_u32 s26, 2457600, s15\n"
    "s_mov_b32 s27, 0x20000\n"
    "s_mul_i32 s52, s65, 1920\n"
    "s_waitcnt lgkmcnt(0)\n"
    "s_add_u32 s24, s32, s15\n"
    "s_addc_u32 s25, s33, 0\n"
    "s_add_u32 m0, s13, 0\n"
    "s_nop 0\n"
    "buffer_load_dwordx4 v32, s[24:27], s52 offen lds\n"
    "s_cmp_ge_u32 s4, 1\n"
    "s_cselect_b32 s17, -1, 4\n"
    "s_add_u32 s17, s17, s4\n"
    "s_lshl_b32 s17, s17, 6\n"
    "v_or_b32 v11, s17, v1\n"
    "v_mul_u32_u24 v12, 0x3334, v11\n"
    "v_lshrrev_b32 v12, 16, v12\n"
    "v_mul_u32_u24 v13, 5, v12\n"
    "v_sub_u32 v11, v11, v13\n"
    "v_bfe_u32 v13, v12, 1, 2\n"
    "v_and_or_b32 v13, v12, 56, v13\n"
    "v_and_b32 v12, 1, v12\n"
    "v_lshl_or_b32 v13, v12, 2, v13\n"
    "v_mul_u32_u24 v13, 0xf00, v13\n"
    "v_lshl_add_u32 v33, v11, 4, v13\n"
    "s_add_u32 s16, s65, 128\n"
    "s_cmp_eq_u32 s16, 1280\n"
    "s_cselect_b32 s66, 0, s16\n"
    "s_cmp_ge_u32 s4, 1\n"
    "s_cselect_b32 s16, s66, s65\n"
    "s_mul_i32 s53, s16, 1920\n"
    "s_add_u32 m0, s13, 4096\n"
    "s_nop 0\n"
    "buffer_load_dwordx4 v33, s[24:27], s53 offen lds\n"
    "s_cmp_ge_u32 s4, 2\n"
    "s_cselect_b32 s17, -2, 3\n"
    "s_add_u32 s17, s17, s4\n"
    "s_lshl_b32 s17, s17, 6\n"
    "v_or_b32 v11, s17, v1\n"
    "v_mul_u32_u24 v12, 0x3334, v11\n"
    "v_lshrrev_b32 v12, 16, v12\n"
    "v_mul_u32_u24 v13, 5, v12\n"
    "v_sub_u32 v11, v11, v13\n"
    "v_bfe_u32 v13, v12, 1, 2\n"
    "v_and_or_b32 v13, v12, 56, v13\n"
    "v_and_b32 v12, 1, v12\n"
    "v_lshl_or_b32 v13, v12, 2, v13\n"
    "v_mul_u32_u24 v13, 0xf00, v13\n"
    "v_lshl_add_u32 v34, v11, 4, v13\n"
    "s_add_u32 s16, s66, 128\n"
    "s_cmp_eq_u32 s16, 1280\n"
    "s_cselect_b32 s67, 0, s16\n"
    "s_cmp_ge_u32 s4, 2\n"
    "s_cselect_b32 s16, s67, s66\n"
    "s_mul_i32 s54, s16, 1920\n"
    "s_add_u32 m0, s13, 8192\n"
    "s_nop 0\n"
    "buffer_load_dwordx4 v34, s[24:27], s54 offen lds\n"
    "s_getpc_b64 s[56:57]\n"
    "s_mul_i32 s15, s6, 768\n"
    "s_mov_b32 s58, 19968\n"
    "s_mov_b32 s59, 0x20000\n"
    "v_lshlrev_b32 v11, 7, v1\n"
    "v_min_u32 v11, 640, v11\n"
    "s_cmp_eq_u32 s4, 3\n"
    "s_cselect_b32 s15, s15, 0x7ffffff0\n"
    "buffer_load_dword v20, v11, s[56:59], s15 offen\n"
    "s_cmp_ge_u32 s4, 3\n"
    "s_cselect_b32 s17, -3, 2\n"
    "s_add_u32 s17, s17, s4\n"
    "s_lshl_b32 s17, s17, 6\n"
    "v_or_b32 v11, s17, v1\n"
    "v_mul_u32_u24 v12, 0x3334, v11\n"
    "v_lshrrev_b32 v12, 16, v12\n"
    "v_mul_u32_u24 v13, 5, v12\n"
    "v_sub_u32 v11, v11, v13\n"
    "v_bfe_u32 v13, v12, 1, 2\n"
    "v_and_or_b32 v13, v12, 56, v13\n"
    "v_and_b32 v12, 1, v12\n"
    "v_lshl_or_b32 v13, v12, 2, v13\n"
    "v_mul_u32_u24 v13, 0xf00, v13\n"
    "v_lshl_add_u32 v35, v11, 4, v13\n"
    "v_lshrrev_b32 v11, 3, v1\n"
    "v_lshrrev_b32 v12, 4, v1\n"
    "v_xor_b32 v12, v12, v1\n"
    "v_and_b32 v12, 7, v12\n"
    "v_mul_u32_u24 v11, 0x500, v11\n"
    "v_lshl_or_b32 v7, v12, 4, v11\n"
    "s_lshl_b32 s16, s7, 2\n"
    "v_xor_b32 v8, 64, v7\n"
    "s_add_u32 s16, s16, s4\n"
    "v_add_u32 v8, 9216, v8\n"
    "s_mul_i32 s14, s16, 40960\n"
    "v_add_u32 v9, 18432, v7\n"
    "v_add_u32 v10, 18432, v8\n"
    "s_mov_b32 s22, 2621440\n"
    "s_mov_b32 s23, 0x20000\n"
    "s_add_u32 s16, s67, 128\n"
    "s_cmp_eq_u32 s16, 1280\n"
    "s_cselect_b32 s68, 0, s16\n"
    "s_cmp_ge_u32 s4, 3\n"
    "s_cselect_b32 s16, s68, s67\n"
    "s_mul_i32 s55, s16, 1920\n"
    "s_lshl_b32 s17, s4, 6\n"
    "s_add_u32 s17, s17, 64\n"
    "v_or_b32 v11, s17, v1\n"
    "v_mul_u32_u24 v12, 0x3334, v11\n"
    "v_lshrrev_b32 v12, 16, v12\n"
    "v_mul_u32_u24 v13, 5, v12\n"
    "v_sub_u32 v11, v11, v13\n"
    "v_bfe_u32 v13, v12, 1, 2\n"
    "v_and_or_b32 v13, v12, 56, v13\n"
    "v_and_b32 v12, 1, v12\n"
    "v_lshl_or_b32 v13, v12, 2, v13\n"
    "v_mul_u32_u24 v13, 0xf00, v13\n"
    "v_lshl_add_u32 v36, v11, 4, v13\n"
    "s_waitcnt vmcnt(2)\n"
    "s_barrier\n"
    "s_add_u32 s15, s67, s14\n"
    "s_add_u32 m0, s84, 135168\n"
    "s_nop 0\n"
    "buffer_load_dwordx4 v7, s[20:23], s15 offen offset:0 lds\n"
    "buffer_load_dwordx4 v8, s[20:23], s15 offen offset:1024 lds\n"
    "buffer_load_dwordx4 v9, s[20:23], s15 offen offset:2048 lds\n"
    "buffer_load_dwordx4 v10, s[20:23], s15 offen offset:3072 lds\n"
    "s_add_u32 m0, s13, 12288\n"
    "s_nop 0\n"
    "buffer_load_dwordx4 v35, s[24:27], s55 offen lds\n"
    "s_add_u32 s15, s68, s14\n"
    "s_add_u32 m0, s84, 86016\n"
    "s_nop 0\n"
    "buffer_load_dwordx4 v7, s[20:23], s15 offen offset:0 lds\n"
    "buffer_load_dwordx4 v8, s[20:23], s15 offen offset:1024 lds\n"
    "buffer_load_dwordx4 v9, s[20:23], s15 offen offset:2048 lds\n"
    "buffer_load_dwordx4 v10, s[20:23], s15 offen offset:3072 lds\n"
    "s_add_u32 s16, s68, 128\n"
    "s_cmp_eq_u32 s16, 1280\n"
    "s_cselect_b32 s69, 0, s16\n"
    "s_mul_i32 s56, s68, 1920\n"
    "s_add_u32 m0, s13, 16384\n"
    "s_nop 0\n"
    "buffer_load_dwordx4 v36, s[24:27], s56 offen lds\n"
    "s_mov_b32 s38, 3522560\n"
    "s_mov_b32 s39, 0x20000\n"
    "s_mov_b32 s42, 25600\n"
    "s_mov_b32 s43, 0x20000\n"
    "v_mov_b32 v11, 0\n"
    "v_lshlrev_b32 v14, 4, v1\n"
    "buffer_load_dwordx2 v[110:111], v11, s[36:39], 0 offen sc1\n"
    "v_add_u32 v11, s13, v14\n"
    "v_mov_b32 v12, 0x7ffffff0\n"
    "v_cmp_gt_u32 vcc, 3200, v11\n"
    "s_lshr_b32 s15, s8, 1\n"
    "s_mul_i32 s15, s15, 3200\n"
    "v_cndmask_b32 v13, v12, v11, vcc\n"
    "s_add_u32 m0, s13, 155648\n"
    "v_cmp_gt_u32 vcc, 3360, v11\n"
    "buffer_load_dwordx4 v13, s[40:43], s15 offen lds\n"
    "s_nop 0\n"
    "v_cndmask_b32 v23, v12, v11, vcc\n"
    "s_waitcnt vmcnt(13)\n"
    "s_barrier\n"
    "s_add_u32 s15, s69, s14\n"
    "s_add_u32 m0, s84, 102400\n"
    "s_nop 0\n"
    "buffer_load_dwordx4 v7, s[20:23], s15 offen offset:0 lds\n"
    "buffer_load_dwordx4 v8, s[20:23], s15 offen offset:1024 lds\n"
    "buffer_load_dwordx4 v9, s[20:23], s15 offen offset:2048 lds\n"
    "buffer_load_dwordx4 v10, s[20:23], s15 offen offset:3072 lds\n"
    "s_add_u32 s16, s69, 128\n"
    "s_cmp_eq_u32 s16, 1280\n"
    "s_cselect_b32 s70, 0, s16\n"
    "s_mul_i32 s57, s69, 1920\n"
    "s_add_u32 m0, s13, 20480\n"
    "s_nop 0\n"
    "buffer_load_dwordx4 v32, s[24:27], s57 offen lds\n"
    "s_cmp_ge_u32 s4, 1\n"
    "s_cselect_b32 s16, s70, s69\n"
    "s_mul_i32 s58, s16, 1920\n"
    "s_add_u32 m0, s13, 24576\n"
    "s_nop 0\n"
    "buffer_load_dwordx4 v33, s[24:27], s58 offen lds\n"
    "s_xor_b32 s9, s8, 1\n"
    "s_lshl_b32 s85, s8, 4\n"
    "s_add_u32 s85, s85, s7\n"
    "s_xor_b32 s96, s85, 16\n"
    "s_mul_i32 s85, s85, 3456\n"
    "s_add_u32 s85, s85, 16384\n"
    "s_mul_i32 s96, s96, 3456\n"
    "s_add_u32 s96, s96, 16384\n"
    "s_lshl_b32 s15, s7, 2\n"
    "s_add_u32 s15, s15, s4\n"
    "s_mul_i32 s15, s15, 160\n"
    "s_lshr_b32 s16, s8, 1\n"
    "s_mul_i32 s16, s16, 10\n"
    "s_add_u32 s15, s15, s16\n"
    "s_lshl_b32 s15, s15, 8\n"
    "s_add_u32 s86, s15, 901120\n"
    "v_add_u32 v21, 2048, v14\n"
    "v_add_u32 v11, 19968, v14\n"
    "v_cmp_gt_u32 vcc, 32, v1\n"
    "v_add_u32 v22, 20992, v14\n"
    "s_lshr_b32 s16, s6, 1\n"
    "s_mul_i32 s15, s16, 52\n"
    "s_lshr_b32 s15, s15, 8\n"
    "s_mul_i32 s15, s15, 5\n"
    "s_sub_u32 s92, s16, s15\n"
    "v_cndmask_b32 v21, v11, v21, vcc\n"
    "s_bitcmp1_b32 0xa5, s11\n"
    "s_cselect_b32 s92, s92, 99\n"
    "v_mov_b32 v16, -1\n"
    "v_mov_b32 v17, -1\n"
    "v_mov_b32 v18, -1\n"
    "v_mov_b32 v19, -1\n"
    "s_lshl_b32 s17, s4, 6\n"
    "v_or_b32 v11, s17, v1\n"
    "v_mul_u32_u24 v12, 0x199a, v11\n"
    "v_lshrrev_b32 v12, 16, v12\n"
    "v_mul_u32_u24 v13, 10, v12\n"
    "v_sub_u32 v11, v11, v13\n"
    "v_and_b32 v13, 19, v12\n"
    "v_bfe_u32 v124, v12, 2, 1\n"
    "v_lshl_or_b32 v13, v124, 3, v13\n"
    "v_bfe_u32 v124, v12, 3, 1\n"
    "v_lshl_or_b32 v13, v124, 2, v13\n"
    "v_mul_u32_u24 v13, 0x500, v13\n"
    "v_lshl_add_u32 v124, v11, 4, v13\n"
    "s_waitcnt vmcnt(13)\n"
    "s_barrier\n"
    "s_cmp_eq_u32 s92, 0\n"
    "s_cbranch_scc0 L_pz2%=\n"
    "buffer_store_dwordx4 v[16:19], v23, s[36:39], s85 offen offset:0 sc1\n"
    "buffer_store_dwordx4 v[16:19], v23, s[36:39], s96 offen offset:0 sc1\n"
    "buffer_store_dwordx4 v[16:19], v14, s[36:39], s86 offen offset:0 sc1\n"
    "buffer_store_dwordx4 v[16:19], v14, s[36:39], s86 offen offset:1024 sc1\n"
    "buffer_store_dwordx4 v[16:19], v21, s[36:39], s86 offen offset:0 sc1\n"
    "buffer_store_dwordx4 v[16:19], v22, s[36:39], s86 offen offset:0 sc1\n"
    "buffer_store_dwordx4 v[16:19], v22, s[36:39], s86 offen offset:1024 sc1\n"
    "L_pz2%=:\n"
    "s_add_u32 s15, s70, s14\n"
    "s_add_u32 m0, s84, 118784\n"
    "s_nop 0\n"
    "buffer_load_dwordx4 v7, s[20:23], s15 offen offset:0 lds\n"
    "buffer_load_dwordx4 v8, s[20:23], s15 offen offset:1024 lds\n"
    "buffer_load_dwordx4 v9, s[20:23], s15 offen offset:2048 lds\n"
    "buffer_load_dwordx4 v10, s[20:23], s15 offen offset:3072 lds\n"
    "s_add_u32 s16, s70, 128\n"
    "s_cmp_eq_u32 s16, 1280\n"
    "s_cselect_b32 s71, 0, s16\n"
    "s_cmp_ge_u32 s4, 2\n"
    "s_cselect_b32 s16, s71, s70\n"
    "s_mul_i32 s59, s16, 1920\n"
    "s_add_u32 m0, s13, 28672\n"
    "s_nop 0\n"
    "buffer_load_dwordx4 v34, s[24:27], s59 offen lds\n"
    "v_lshrrev_b32 v11, 1, v1\n"
    "v_and_b32 v11, 30, v11\n"
    "s_lshr_b32 s15, s4, 1\n"
    "s_lshl_b32 s16, s15, 7\n"
    "v_add_u32 v11, s16, v11\n"
    "v_mul_u32_u24 v105, 80, v11\n"
    "v_mul_u32_u24 v107, 48, v11\n"
    "v_and_b32 v12, 3, v1\n"
    "v_and_b32 v13, 1, v1\n"
    "v_lshl_add_u32 v106, v13, 3, v105\n"
    "v_add_u32 v106, 64, v106\n"
    "v_lshl_add_u32 v105, v12, 3, v105\n"
    "v_or_b32 v13, 4, v13\n"
    "s_bitcmp1_b32 s4, 0\n"
    "s_cselect_b64 s[80:81], -1, 0\n"
    "v_add_u32 v107, 20480, v107\n"
    "v_cndmask_b32 v12, v12, v13, s[80:81]\n"
    "v_lshl_add_u32 v107, v12, 3, v107\n"
    "s_mul_i32 s15, s15, 21\n"
    "s_and_b32 s16, s4, 1\n"
    "s_lshl_b32 s16, s16, 4\n"
    "s_add_u32 s15, s15, s16\n"
    "v_and_b32 v11, 15, v1\n"
    "v_cmp_lt_u32 vcc, 4, v11\n"
    "v_add_u32 v11, s15, v11\n"
    "s_and_b64 s[80:81], vcc, s[80:81]\n"
    "v_mul_u32_u24 v11, 80, v11\n"
    "v_bfe_u32 v12, v1, 4, 1\n"
    "v_lshrrev_b32 v13, 5, v1\n"
    "v_lshl_add_u32 v108, v12, 5, v11\n"
    "v_lshl_add_u32 v108, v13, 4, v108\n"
    "v_lshl_add_u32 v109, v12, 3, v11\n"
    "v_add_u32 v109, 64, v109\n"
    "v_mov_b32 v11, 0x7ffffff0\n"
    "v_cmp_lt_u32 vcc, 31, v1\n"
    "v_cndmask_b32 v108, v108, v11, s[80:81]\n"
    "s_or_b64 s[80:81], vcc, s[80:81]\n"
    "v_cndmask_b32 v109, v109, v11, s[80:81]\n"
    "s_waitcnt vmcnt(13)\n"
    "s_barrier\n"
    "s_cmp_eq_u32 s92, 1\n"
    "s_cbranch_scc0 L_pz3%=\n"
    "buffer_store_dwordx4 v[16:19], v23, s[36:39], s85 offen offset:0 sc1\n"
    "buffer_store_dwordx4 v[16:19], v23, s[36:39], s96 offen offset:0 sc1\n"
    "buffer_store_dwordx4 v[16:19], v14, s[36:39], s86 offen offset:0 sc1\n"
    "buffer_store_dwordx4 v[16:19], v14, s[36:39], s86 offen offset:1024 sc1\n"
    "buffer_store_dwordx4 v[16:19], v21, s[36:39], s86 offen offset:0 sc1\n"
    "buffer_store_dwordx4 v[16:19], v22, s[36:39], s86 offen offset:0 sc1\n"
    "buffer_store_dwordx4 v[16:19], v22, s[36:39], s86 offen offset:1024 sc1\n"
    "L_pz3%=:\n"
    "s_add_u32 s15, s71, s14\n"
    "s_add_u32 m0, s84, 135168\n"
    "s_nop 0\n"
    "buffer_load_dwordx4 v7, s[20:23], s15 offen offset:0 lds\n"
    "buffer_load_dwordx4 v8, s[20:23], s15 offen offset:1024 lds\n"
    "buffer_load_dwordx4 v9, s[20:23], s15 offen offset:2048 lds\n"
    "buffer_load_dwordx4 v10, s[20:23], s15 offen offset:3072 lds\n"
    "s_add_u32 s16, s71, 128\n"
    "s_cmp_eq_u32 s16, 1280\n"
    "s_cselect_b32 s72, 0, s16\n"
    "s_cmp_ge_u32 s4, 3\n"
    "s_cselect_b32 s16, s72, s71\n"
    "s_mul_i32 s60, s16, 1920\n"
    "s_add_u32 m0, s13, 32768\n"
    "s_nop 0\n"
    "buffer_load_dwordx4 v35, s[24:27], s60 offen lds\n"
    "s_cmp_ge_u32 s4, 1\n"
    "s_cselect_b32 s17, -1, 4\n"
    "s_add_u32 s17, s17, s4\n"
    "s_lshl_b32 s17, s17, 6\n"
    "v_or_b32 v11, s17, v1\n"
    "v_mul_u32_u24 v12, 0x199a, v11\n"
    "v_lshrrev_b32 v12, 16, v12\n"
    "v_mul_u32_u24 v13, 10, v12\n"
    "v_sub_u32 v11, v11, v13\n"
    "v_and_b32 v13, 19, v12\n"
    "v_bfe_u32 v125, v12, 2, 1\n"
    "v_lshl_or_b32 v13, v125, 3, v13\n"
    "v_bfe_u32 v125, v12, 3, 1\n"
    "v_lshl_or_b32 v13, v125, 2, v13\n"
    "v_mul_u32_u24 v13, 0x500, v13\n"
    "v_lshl_add_u32 v125, v11, 4, v13\n"
    "s_cmp_ge_u32 s4, 2\n"
    "s_cselect_b32 s17, -2, 3\n"
    "s_add_u32 s17, s17, s4\n"
    "s_lshl_b32 s17, s17, 6\n"
    "v_or_b32 v11, s17, v1\n"
    "v_mul_u32_u24 v12, 0x199a, v11\n"
    "v_lshrrev_b32 v12, 16, v12\n"
    "v_mul_u32_u24 v13, 10, v12\n"
    "v_sub_u32 v11, v11, v13\n"
    "v_and_b32 v13, 19, v12\n"
    "v_bfe_u32 v126, v12, 2, 1\n"
    "v_lshl_or_b32 v13, v126, 3, v13\n"
    "v_bfe_u32 v126, v12, 3, 1\n"
    "v_lshl_or_b32 v13, v126, 2, v13\n"
    "v_mul_u32_u24 v13, 0x500, v13\n"
    "v_lshl_add_u32 v126, v11, 4, v13\n"
    "s_waitcnt vmcnt(10)\n"
    "s_barrier\n"
    "s_cmp_eq_u32 s92, 2\n"
    "s_cbranch_scc0 L_pz4%=\n"
    "buffer_store_dwordx4 v[16:19], v23, s[36:39], s85 offen offset:0 sc1\n"
    "buffer_store_dwordx4 v[16:19], v23, s[36:39], s96 offen offset:0 sc1\n"
    "buffer_store_dwordx4 v[16:19], v14, s[36:39], s86 offen offset:0 sc1\n"
    "buffer_store_dwordx4 v[16:19], v14, s[36:39], s86 offen offset:1024 sc1\n"
    "buffer_store_dwordx4 v[16:19], v21, s[36:39], s86 offen offset:0 sc1\n"
    "buffer_store_dwordx4 v[16:19], v22, s[36:39], s86 offen offset:0 sc1\n"
    "buffer_store_dwordx4 v[16:19], v22, s[36:39], s86 offen offset:1024 sc1\n"
    "L_pz4%=:\n"
    "s_add_u32 s15, s72, s14\n"
    "s_add_u32 m0, s84, 86016\n"
    "s_nop 0\n"
    "buffer_load_dwordx4 v7, s[20:23], s15 offen offset:0 lds\n"
    "buffer_load_dwordx4 v8, s[20:23], s15 offen offset:1024 lds\n"
    "buffer_load_dwordx4 v9, s[20:23], s15 offen offset:2048 lds\n"
    "buffer_load_dwordx4 v10, s[20:23], s15 offen offset:3072 lds\n"
    "s_add_u32 s16, s72, 128\n"
    "s_cmp_eq_u32 s16, 1280\n"
    "s_cselect_b32 s73, 0, s16\n"
    "s_mul_i32 s61, s72, 1920\n"
    "s_add_u32 m0, s13, 36864\n"
    "s_nop 0\n"
    "buffer_load_dwordx4 v36, s[24:27], s61 offen lds\n"
    "v_readfirstlane_b32 s15, v110\n"
    "v_readfirstlane_b32 s16, v111\n"
    "s_mul_i32 s76, s15, 0x4c957f2d\n"
    "s_mul_hi_u32 s77, s15, 0x4c957f2d\n"
    "s_mul_i32 s15, s15, 0x5851f42d\n"
    "s_mul_i32 s16, s16, 0x4c957f2d\n"
    "s_add_u32 s77, s77, s15\n"
    "s_add_u32 s77, s77, s16\n"
    "s_add_u32 s76, s76, 0xf767814f\n"
    "s_addc_u32 s77, s77, 0x14057b7e\n"
    "v_cmp_eq_u32 vcc, 0, v1\n"
    "v_mov_b32 v11, 0x7ffffff0\n"
    "v_lshlrev_b32 v14, 4, v1\n"
    "v_cndmask_b32 v15, v11, v1, vcc\n"
    "s_cmp_ge_u32 s4, 3\n"
    "s_cselect_b32 s17, -3, 2\n"
    "s_add_u32 s17, s17, s4\n"
    "s_lshl_b32 s17, s17, 6\n"
    "v_or_b32 v11, s17, v1\n"
    "v_mul_u32_u24 v12, 0x199a, v11\n"
    "v_lshrrev_b32 v12, 16, v12\n"
    "v_mul_u32_u24 v13, 10, v12\n"
    "v_sub_u32 v11, v11, v13\n"
    "v_and_b32 v13, 19, v12\n"
    "v_bfe_u32 v127, v12, 2, 1\n"
    "v_lshl_or_b32 v13, v127, 3, v13\n"
    "v_bfe_u32 v127, v12, 3, 1\n"
    "v_lshl_or_b32 v13, v127, 2, v13\n"
    "v_mul_u32_u24 v13, 0x500, v13\n"
    "v_lshl_add_u32 v127, v11, 4, v13\n"
    "s_waitcnt vmcnt(10)\n"
    "s_barrier\n"
    "s_cmp_eq_u32 s92, 3\n"
    "s_cbranch_scc0 L_pz5%=\n"
    "buffer_store_dwordx4 v[16:19], v23, s[36:39], s85 offen offset:0 sc1\n"
    "buffer_store_dwordx4 v[16:19], v23, s[36:39], s96 offen offset:0 sc1\n"
    "buffer_store_dwordx4 v[16:19], v14, s[36:39], s86 offen offset:0 sc1\n"
    "buffer_store_dwordx4 v[16:19], v14, s[36:39], s86 offen offset:1024 sc1\n"
    "buffer_store_dwordx4 v[16:19], v21, s[36:39], s86 offen offset:0 sc1\n"
    "buffer_store_dwordx4 v[16:19], v22, s[36:39], s86 offen offset:0 sc1\n"
    "buffer_store_dwordx4 v[16:19], v22, s[36:39], s86 offen offset:1024 sc1\n"
    "L_pz5%=:\n"
    "s_add_u32 s15, s73, s14\n"
    "s_add_u32 m0, s84, 102400\n"
    "s_nop 0\n"
    "buffer_load_dwordx4 v7, s[20:23], s15 offen offset:0 lds\n"
    "buffer_load_dwordx4 v8, s[20:23], s15 offen offset:1024 lds\n"
    "buffer_load_dwordx4 v9, s[20:23], s15 offen offset:2048 lds\n"
    "buffer_load_dwordx4 v10, s[20:23], s15 offen offset:3072 lds\n"
    "s_add_u32 s16, s73, 128\n"
    "s_cmp_eq_u32 s16, 1280\n"
    "s_cselect_b32 s74, 0, s16\n"
    "s_mul_i32 s62, s73, 1920\n"
    "s_add_u32 m0, s13, 40960\n"
    "s_nop 0\n"
    "buffer_load_dwordx4 v32, s[24:27], s62 offen lds\n"
    "s_cmp_ge_u32 s4, 1\n"
    "s_cselect_b32 s16, s74, s73\n"
    "s_mul_i32 s63, s16, 1920\n"
    "s_add_u32 m0, s13, 45056\n"
    "s_nop 0\n"
    "buffer_load_dwordx4 v33, s[24:27], s63 offen lds\n"
    "s_add_u32 s28, s24, 1280\n"
    "s_addc_u32 s29, s25, 0\n"
    "s_sub_u32 s30, s26, 1280\n"
    "s_mov_b32 s31, 0x20000\n"
    "s_mul_i32 s15, s9, 80\n"
    "s_add_u32 s32, s32, s15\n"
    "s_addc_u32 s33, s33, 0\n"
    "s_sub_u32 s34, 2457600, s15\n"
    "s_mov_b32 s35, 0x20000\n"
    "s_add_u32 s16, s13, 151552\n"
    "s_add_u32 s15, s13, 49152\n"
    "s_cmp_lt_u32 s4, 2\n"
    "s_cselect_b32 s93, s15, s16\n"
    "s_add_u32 s15, s13, 100352\n"
    "s_cmp_lt_u32 s4, 2\n"
    "s_cselect_b32 s94, s15, s16\n"
    "s_add_u32 s15, s13, 151552\n"
    "s_cmp_lt_u32 s4, 2\n"
    "s_cselect_b32 s95, s15, s16\n"
    "v_and_b32 v11, 15, v1\n"
    "v_lshrrev_b32 v12, 4, v1\n"
    "s_lshl_b32 s15, s4, 4\n"
    "v_add_u32 v13, s15, v11\n"
    "v_mul_u32_u24 v112, 3456, v12\n"
    "v_lshl_add_u32 v112, v13, 4, v112\n"
    "v_add_u32 v113, 1024, v112\n"
    "v_add_u32 v114, 2048, v112\n"
    "v_add_u32 v115, 3072, v112\n"
    "v_cmp_gt_u32 vcc, 18, v13\n"
    "v_mov_b32 v117, 0x7ffffff0\n"
    "v_lshlrev_b32 v116, 4, v13\n"
    "v_cndmask_b32 v115, v117, v115, vcc\n"
    "v_lshl_add_u32 v116, v12, 3, v116\n"
    "v_add_u32 v116, 159744, v116\n"
    "v_lshlrev_b32 v13, 3, v1\n"
    "v_add_u32 v13, 153600, v13\n"
    "v_cmp_gt_u32 s[80:81], 32, v1\n"
    "v_add_u32 v117, 1024, v116\n"
    "v_add_u32 v118, 2048, v116\n"
    "v_add_u32 v119, 3072, v116\n"
    "s_cmp_lt_u32 s4, 2\n"
    "s_cselect_b64 s[82:83], s[80:81], 0\n"
    "v_cndmask_b32 v116, v13, v116, s[80:81]\n"
    "v_cndmask_b32 v117, v13, v117, s[80:81]\n"
    "v_cndmask_b32 v118, v13, v118, s[80:81]\n"
    "v_cndmask_b32 v119, v13, v119, s[82:83]\n"
    "s_waitcnt vmcnt(11)\n"
    "s_barrier\n"
    "s_cmp_eq_u32 s92, 4\n"
    "s_cbranch_scc0 L_pz6%=\n"
    "buffer_store_dwordx4 v[16:19], v23, s[36:39], s85 offen offset:0 sc1\n"
    "buffer_store_dwordx4 v[16:19], v23, s[36:39], s96 offen offset:0 sc1\n"
    "buffer_store_dwordx4 v[16:19], v14, s[36:39], s86 offen offset:0 sc1\n"
    "buffer_store_dwordx4 v[16:19], v14, s[36:39], s86 offen offset:1024 sc1\n"
    "buffer_store_dwordx4 v[16:19], v21, s[36:39], s86 offen offset:0 sc1\n"
    "buffer_store_dwordx4 v[16:19], v22, s[36:39], s86 offen offset:0 sc1\n"
    "buffer_store_dwordx4 v[16:19], v22, s[36:39], s86 offen offset:1024 sc1\n"
    "L_pz6%=:\n"
    "s_add_u32 s15, s74, s14\n"
    "s_add_u32 m0, s84, 118784\n"
    "s_nop 0\n"
    "buffer_load_dwordx4 v7, s[20:23], s15 offen offset:0 lds\n"
    "buffer_load_dwordx4 v8, s[20:23], s15 offen offset:1024 lds\n"
    "buffer_load_dwordx4 v9, s[20:23], s15 offen offset:2048 lds\n"
    "buffer_load_dwordx4 v10, s[20:23], s15 offen offset:3072 lds\n"
    "s_mul_i32 s16, s74, 1920\n"
    "s_cmp_lt_u32 s4, 2\n"
    "s_cselect_b32 s64, s16, 0x7ffffff0\n"
    "s_mov_b32 m0, s93\n"
    "s_nop 0\n"
    "buffer_load_dwordx4 v34, s[24:27], s64 offen lds\n"
    "v_and_b32 v11, 15, v1\n"
    "v_and_b32 v12, 1, v1\n"
    "v_lshlrev_b32 v12, 4, v12\n"
    "v_mov_b32 v13, 0x3f80\n"
    "v_lshlrev_b32 v13, v12, v13\n"
    "v_lshrrev_b32 v11, 1, v11\n"
    "v_cmp_eq_u32 vcc, 0, v11\n"
    "s_mul_i32 s16, s9, 55296\n"
    "s_add_u32 s87, s16, 16384\n"
    "v_cndmask_b32 v120, 0, v13, vcc\n"
    "v_cmp_eq_u32 vcc, 1, v11\n"
    "s_mul_i32 s16, s9, 55296\n"
    "s_add_u32 s88, s16, 30208\n"
    "v_cndmask_b32 v121, 0, v13, vcc\n"
    "v_cmp_eq_u32 vcc, 2, v11\n"
    "s_mul_i32 s16, s9, 55296\n"
    "s_add_u32 s89, s16, 44032\n"
    "v_cndmask_b32 v122, 0, v13, vcc\n"
    "v_cmp_eq_u32 vcc, 3, v11\n"
    "s_mul_i32 s16, s9, 55296\n"
    "s_add_u32 s90, s16, 57856\n"
    "v_cndmask_b32 v123, 0, v13, vcc\n"
    "s_waitcnt vmcnt(11)\n"
    "s_barrier\n"
    "s_add_u32 m0, s13, 51200\n"
    "s_nop 0\n"
    "buffer_load_dwordx4 v32, s[28:31], s52 offen lds\n"
    "s_add_u32 m0, s13, 55296\n"
    "s_nop 0\n"
    "buffer_load_dwordx4 v33, s[28:31], s53 offen lds\n"
    "s_lshl_b32 s17, s4, 6\n"
    "s_add_u32 s17, s17, 64\n"
    "v_or_b32 v11, s17, v1\n"
    "v_mul_u32_u24 v12, 0x199a, v11\n"
    "v_lshrrev_b32 v12, 16, v12\n"
    "v_mul_u32_u24 v13, 10, v12\n"
    "v_sub_u32 v11, v11, v13\n"
    "v_and_b32 v13, 19, v12\n"
    "v_bfe_u32 v7, v12, 2, 1\n"
    "v_lshl_or_b32 v13, v7, 3, v13\n"
    "v_bfe_u32 v7, v12, 3, 1\n"
    "v_lshl_or_b32 v13, v7, 2, v13\n"
    "v_mul_u32_u24 v13, 0x500, v13\n"
    "v_lshl_add_u32 v7, v11, 4, v13\n"
    "s_lshr_b32 s15, s6, 3\n"
    "s_lshl_b32 s10, s5, 2\n"
    "s_add_u32 s10, s10, s15\n"
    "s_mul_i32 s15, s15, 3\n"
    "s_add_u32 s15, s15, s11\n"
    "s_add_u32 s15, s15, s5\n"
    "s_mul_i32 s16, s15, 26\n"
    "s_lshr_b32 s16, s16, 8\n"
    "s_mul_i32 s16, s16, 10\n"
    "s_sub_u32 s12, s15, s16\n"
    "s_lshr_b32 s15, s8, 1\n"
    "s_lshl_b32 s15, s15, 7\n"
    "s_add_u32 s15, s15, 4096\n"
    "s_lshr_b32 s16, s10, 1\n"
    "s_lshl_b32 s16, s16, 6\n"
    "s_add_u32 s16, s16, 8064\n"
    "v_lshlrev_b32 v11, 3, v1\n"
    "v_add_u32 v8, s15, v11\n"
    "v_add_u32 v11, s16, v11\n"
    "v_cmp_gt_u32 vcc, 16, v1\n"
    "v_mov_b32 v12, 0x7ffffff0\n"
    "s_nop 0\n"
    "v_cndmask_b32 v8, v11, v8, vcc\n"
    "v_cmp_gt_u32 vcc, 24, v1\n"
    "s_nop 1\n"
    "v_cndmask_b32 v8, v12, v8, vcc\n"
    "s_waitcnt vmcnt(7)\n"
    "s_barrier\n"
    "s_add_u32 m0, s13, 59392\n"
    "s_nop 0\n"
    "buffer_load_dwordx4 v34, s[28:31], s54 offen lds\n"
    "s_add_u32 m0, s13, 63488\n"
    "s_nop 0\n"
    "buffer_load_dwordx4 v35, s[28:31], s55 offen lds\n"
    "v_bfe_u32 v11, v1, 2, 2\n"
    "v_bfe_u32 v12, v1, 4, 1\n"
    "v_lshrrev_b32 v13, 5, v1\n"
    "v_lshl_or_b32 v11, v12, 2, v11\n"
    "v_lshl_or_b32 v11, v13, 4, v11\n"
    "v_mul_u32_u24 v11, 160, v11\n"
    "v_and_b32 v2, 3, v1\n"
    "v_lshl_add_u32 v2, v2, 3, v11\n"
    "v_add_u32 v2, 51200, v2\n"
    "v_and_b32 v11, 15, v1\n"
    "v_mul_u32_u24 v11, 0x500, v11\n"
    "v_lshl_add_u32 v3, v12, 5, v11\n"
    "v_lshl_add_u32 v3, v13, 4, v3\n"
    "v_lshrrev_b32 v12, 4, v1\n"
    "v_lshl_add_u32 v4, v12, 3, v11\n"
    "s_mul_i32 s15, s4, 5120\n"
    "s_add_u32 s15, s15, 102400\n"
    "v_add_u32 v5, s15, v14\n"
    "s_lshl_b32 s15, s10, 2\n"
    "s_add_u32 s15, s15, s4\n"
    "s_mul_i32 s14, s15, 20480\n"
    "s_lshl_b32 s65, s12, 10\n"
    "s_waitcnt vmcnt(4)\n"
    "s_barrier\n"
    "s_add_u32 m0, s13, 67584\n"
    "s_nop 0\n"
    "buffer_load_dwordx4 v36, s[28:31], s56 offen lds\n"
    "s_add_u32 m0, s13, 71680\n"
    "s_nop 0\n"
    "buffer_load_dwordx4 v32, s[28:31], s57 offen lds\n"
    "s_lshr_b32 s17, s8, 1\n"
    "s_lshl_b32 s15, s17, 4\n"
    "s_add_u32 s15, s15, s7\n"
    "s_lshl_b32 s15, s15, 3\n"
    "s_add_u32 s15, s15, 4096\n"
    "s_lshl_b32 s16, s7, 3\n"
    "s_add_u32 s16, s16, s17\n"
    "s_lshl_b32 s16, s16, 3\n"
    "s_add_u32 s16, s16, 8192\n"
    "s_cmp_eq_u32 s4, 0\n"
    "s_cselect_b32 s15, s15, s16\n"
    "s_cmp_lt_u32 s4, 2\n"
    "s_cselect_b32 s15, s15, 0x7ffffff0\n"
    "v_mov_b32 v110, s76\n"
    "v_mov_b32 v111, s77\n"
    "s_cmp_eq_u32 s92, 99\n"
    "s_cbranch_scc1 L_nf%=\n"
    "buffer_store_dwordx2 v[110:111], v15, s[36:39], s15 offen sc1\n"
    "L_nf%=:\n"
    "s_add_u32 m0, s13, 75776\n"
    "s_nop 0\n"
    "buffer_load_dwordx4 v33, s[28:31], s58 offen lds\n"
    "s_add_u32 m0, s13, 79872\n"
    "s_nop 0\n"
    "buffer_load_dwordx4 v34, s[28:31], s59 offen lds\n"
    "s_add_u32 m0, s13, 83968\n"
    "s_nop 0\n"
    "buffer_load_dwordx4 v35, s[28:31], s60 offen lds\n"
    "s_add_u32 m0, s13, 88064\n"
    "s_nop 0\n"
    "buffer_load_dwordx4 v36, s[28:31], s61 offen lds\n"
    "s_add_u32 m0, s13, 92160\n"
    "s_nop 0\n"
    "buffer_load_dwordx4 v32, s[28:31], s62 offen lds\n"
    "s_mul_i32 s15, s11, 160\n"
    "s_add_u32 s44, s44, s15\n"
    "s_addc_u32 s45, s45, 0\n"
    "s_sub_u32 s46, 819200, s15\n"
    "s_mov_b32 s47, 0x20000\n"
    "s_mul_i32 s20, s12, 40960\n"
    "s_add_u32 s16, s20, 40960\n"
    "s_cmp_eq_u32 s16, 409600\n"
    "s_cselect_b32 s21, 0, s16\n"
    "s_add_u32 s16, s21, 40960\n"
    "s_cmp_eq_u32 s16, 409600\n"
    "s_cselect_b32 s22, 0, s16\n"
    "s_add_u32 s16, s22, 40960\n"
    "s_cmp_eq_u32 s16, 409600\n"
    "s_cselect_b32 s23, 0, s16\n"
    "s_add_u32 s16, s23, 40960\n"
    "s_cmp_eq_u32 s16, 409600\n"
    "s_cselect_b32 s24, 0, s16\n"
    "s_add_u32 s16, s24, 40960\n"
    "s_cmp_eq_u32 s16, 409600\n"
    "s_cselect_b32 s25, 0, s16\n"
    "s_add_u32 s16, s25, 40960\n"
    "s_cmp_eq_u32 s16, 409600\n"
    "s_cselect_b32 s26, 0, s16\n"
    "s_add_u32 s16, s26, 40960\n"
    "s_cmp_eq_u32 s16, 409600\n"
    "s_cselect_b32 s27, 0, s16\n"
    "s_add_u32 s16, s27, 40960\n"
    "s_cmp_eq_u32 s16, 409600\n"
    "s_cselect_b32 s40, 0, s16\n"
    "s_add_u32 s16, s40, 40960\n"
    "s_cmp_eq_u32 s16, 409600\n"
    "s_cselect_b32 s41, 0, s16\n"
    "s_add_u32 s16, s65, 1024\n"
    "s_cmp_eq_u32 s16, 10240\n"
    "s_cselect_b32 s66, 0, s16\n"
    "s_add_u32 s16, s66, 1024\n"
    "s_cmp_eq_u32 s16, 10240\n"
    "s_cselect_b32 s67, 0, s16\n"
    "s_add_u32 s16, s67, 1024\n"
    "s_cmp_eq_u32 s16, 10240\n"
    "s_cselect_b32 s68, 0, s16\n"
    "s_add_u32 s16, s68, 1024\n"
    "s_cmp_eq_u32 s16, 10240\n"
    "s_cselect_b32 s69, 0, s16\n"
    "s_add_u32 s16, s69, 1024\n"
    "s_cmp_eq_u32 s16, 10240\n"
    "s_cselect_b32 s70, 0, s16\n"
    "s_add_u32 s16, s70, 1024\n"
    "s_cmp_eq_u32 s16, 10240\n"
    "s_cselect_b32 s71, 0, s16\n"
    "s_add_u32 s16, s71, 1024\n"
    "s_cmp_eq_u32 s16, 10240\n"
    "s_cselect_b32 s72, 0, s16\n"
    "s_add_u32 s16, s72, 1024\n"
    "s_cmp_eq_u32 s16, 10240\n"
    "s_cselect_b32 s73, 0, s16\n"
    "s_add_u32 s16, s73, 1024\n"
    "s_cmp_eq_u32 s16, 10240\n"
    "s_cselect_b32 s74, 0, s16\n"
    "s_add_u32 s15, s14, 901120\n"
    "s_add_u32 s65, s65, s15\n"
    "s_add_u32 s66, s66, s15\n"
    "s_add_u32 s67, s67, s15\n"
    "s_add_u32 s68, s68, s15\n"
    "s_add_u32 s69, s69, s15\n"
    "s_add_u32 s70, s70, s15\n"
    "s_add_u32 s71, s71, s15\n"
    "s_add_u32 s72, s72, s15\n"
    "s_add_u32 s73, s73, s15\n"
    "s_add_u32 s74, s74, s15\n"
    "s_mul_i32 s15, s11, 160\n"
    "s_add_u32 s14, s14, s15\n"
    "s_mov_b32 s91, 0\n"
    "s_mov_b32 s50, 2621440\n"
    "s_mov_b32 s51, 0x20000\n"
    "s_waitcnt vmcnt(4)\n"
    "s_barrier\n"
    "s_add_u32 m0, s13, 96256\n"
    "s_nop 0\n"
    "buffer_load_dwordx4 v33, s[28:31], s63 offen lds\n"
    "s_mov_b32 m0, s94\n"
    "s_nop 0\n"
    "buffer_load_dwordx4 v34, s[28:31], s64 offen lds\n"
    "buffer_load_dwordx2 v[110:111], v8, s[36:39], 0 offen sc1\n"
    "s_add_u32 m0, s13, 102400\n"
    "s_nop 0\n"
    "buffer_load_dwordx4 v32, s[32:35], s52 offen lds\n"
    "s_add_u32 m0, s13, 106496\n"
    "s_nop 0\n"
    "buffer_load_dwordx4 v33, s[32:35], s53 offen lds\n"
    "s_add_u32 m0, s13, 110592\n"
    "s_nop 0\n"
    "buffer_load_dwordx4 v34, s[32:35], s54 offen lds\n"
    "s_add_u32 m0, s13, 114688\n"
    "s_nop 0\n"
    "buffer_load_dwordx4 v35, s[32:35], s55 offen lds\n"
    "s_waitcnt vmcnt(6)\n"
    "s_barrier\n"
    "s_add_u32 m0, s13, 118784\n"
    "s_nop 0\n"
    "buffer_load_dwordx4 v36, s[32:35], s56 offen lds\n"
    "s_add_u32 m0, s13, 122880\n"
    "s_nop 0\n"
    "buffer_load_dwordx4 v32, s[32:35], s57 offen lds\n"
    "s_add_u32 m0, s13, 126976\n"
    "s_nop 0\n"
    "buffer_load_dwordx4 v33, s[32:35], s58 offen lds\n"
    "s_add_u32 m0, s13, 131072\n"
    "s_nop 0\n"
    "buffer_load_dwordx4 v34, s[32:35], s59 offen lds\n"
    "s_add_u32 m0, s13, 135168\n"
    "s_nop 0\n"
    "buffer_load_dwordx4 v35, s[32:35], s60 offen lds\n"
    "s_waitcnt vmcnt(10)\n"
    "s_barrier\n"
    "s_waitcnt vmcnt(9)\n"
    "v_cmp_ne_u32 s[80:81], s76, v110\n"
    "v_cmp_ne_u32 vcc, s77, v111\n"
    "s_or_b64 vcc, vcc, s[80:81]\n"
    "s_and_b32 s15, vcc_lo, 0xffffff\n"
    "s_cbranch_scc1 L_f1_slow%=\n"
    "L_f1_done%=:\n"
    "ds_read_b64_tr_b16 a[76:77], v105 offset:0\n"
    "ds_read_b64_tr_b16 a[78:79], v105 offset:80\n"
    "ds_read_b64_tr_b16 a[80:81], v105 offset:32\n"
    "ds_read_b64_tr_b16 a[82:83], v105 offset:112\n"
    "ds_read_b64_tr_b16 a[84:85], v106 offset:0\n"
    "ds_read_b64_tr_b16 a[86:87], v106 offset:80\n"
    "ds_read_b64_tr_b16 a[88:89], v105 offset:2560\n"
    "ds_read_b64_tr_b16 a[90:91], v105 offset:2640\n"
    "ds_read_b64_tr_b16 a[92:93], v105 offset:2592\n"
    "ds_read_b64_tr_b16 a[94:95], v105 offset:2672\n"
    "ds_read_b64_tr_b16 a[96:97], v106 offset:2560\n"
    "ds_read_b64_tr_b16 a[98:99], v106 offset:2640\n"
    "ds_read_b64_tr_b16 a[100:101], v105 offset:5120\n"
    "ds_read_b64_tr_b16 a[102:103], v105 offset:5200\n"
    "ds_read_b64_tr_b16 a[104:105], v105 offset:5152\n"
    "ds_read_b64_tr_b16 a[106:107], v105 offset:5232\n"
    "ds_read_b64_tr_b16 a[108:109], v106 offset:5120\n"
    "ds_read_b64_tr_b16 a[110:111], v106 offset:5200\n"
    "ds_read_b64_tr_b16 a[112:113], v105 offset:7680\n"
    "ds_read_b64_tr_b16 a[114:115], v105 offset:7760\n"
    "ds_read_b64_tr_b16 a[116:117], v105 offset:7712\n"
    "ds_read_b64_tr_b16 a[118:119], v105 offset:7792\n"
    "ds_read_b64_tr_b16 a[120:121], v106 offset:7680\n"
    "ds_read_b64_tr_b16 a[122:123], v106 offset:7760\n"
    "s_add_u32 m0, s13, 139264\n"
    "s_nop 0\n"
    "buffer_load_dwordx4 v36, s[32:35], s61 offen lds\n"
    "s_add_u32 m0, s13, 143360\n"
    "s_nop 0\n"
    "buffer_load_dwordx4 v32, s[32:35], s62 offen lds\n"
    "s_add_u32 m0, s13, 147456\n"
    "s_nop 0\n"
    "buffer_load_dwordx4 v33, s[32:35], s63 offen lds\n"
    "s_mov_b32 m0, s95\n"
    "s_nop 0\n"
    "buffer_load_dwordx4 v34, s[32:35], s64 offen lds\n"
    "s_setprio 2\n"
    "s_waitcnt vmcnt(8)\n"
    "s_barrier\n"
    "ds_read_b64_tr_b16 a[16:17], v107 offset:0\n"
    "ds_read_b64_tr_b16 a[18:19], v107 offset:48\n"
    "ds_read_b64_tr_b16 a[20:21], v107 offset:1536\n"
    "ds_read_b64_tr_b16 a[22:23], v107 offset:1584\n"
    "ds_read_b64_tr_b16 a[24:25], v107 offset:3072\n"
    "ds_read_b64_tr_b16 a[26:27], v107 offset:3120\n"
    "ds_read_b64_tr_b16 a[28:29], v107 offset:4608\n"
    "ds_read_b64_tr_b16 a[30:31], v107 offset:4656\n"
    "s_waitcnt lgkmcnt(6)\n"
    "v_mfma_f32_16x16x32_bf16 v[16:19], a[76:79], a[16:19], 0\n"
    "v_mfma_f32_16x16x32_bf16 v[20:23], a[80:83], a[16:19], 0\n"
    "v_mfma_f32_16x16x32_bf16 v[24:27], a[84:87], a[16:19], 0\n"
    "s_waitcnt lgkmcnt(4)\n"
    "v_mfma_f32_16x16x32_bf16 v[16:19], a[88:91], a[20:23], v[16:19]\n"
    "v_mfma_f32_16x16x32_bf16 v[20:23], a[92:95], a[20:23], v[20:23]\n"
    "v_mfma_f32_16x16x32_bf16 v[24:27], a[96:99], a[20:23], v[24:27]\n"
    "s_waitcnt lgkmcnt(2)\n"
    "v_mfma_f32_16x16x32_bf16 v[16:19], a[100:103], a[24:27], v[16:19]\n"
    "v_mfma_f32_16x16x32_bf16 v[20:23], a[104:107], a[24:27], v[20:23]\n"
    "v_mfma_f32_16x16x32_bf16 v[24:27], a[108:111], a[24:27], v[24:27]\n"
    "s_waitcnt lgkmcnt(0)\n"
    "v_mfma_f32_16x16x32_bf16 v[16:19], a[112:115], a[28:31], v[16:19]\n"
    "v_mfma_f32_16x16x32_bf16 v[20:23], a[116:119], a[28:31], v[20:23]\n"
    "v_mfma_f32_16x16x32_bf16 v[24:27], a[120:123], a[28:31], v[24:27]\n"
    "s_nop 5\n"
    "v_cvt_pk_bf16_f32 v40, v16, v17\n"
    "v_cvt_pk_bf16_f32 v41, v18, v19\n"
    "v_cvt_pk_bf16_f32 v42, v20, v21\n"
    "v_cvt_pk_bf16_f32 v43, v22, v23\n"
    "v_cvt_pk_bf16_f32 v44, v24, v25\n"
    "v_cvt_pk_bf16_f32 v45, v26, v27\n"
    "v_permlane16_swap_b32 v40, v42\n"
    "v_permlane16_swap_b32 v41, v43\n"
    "v_min_u32 v44, -2, v44\n"
    "v_min_u32 v45, -2, v45\n"
    "v_min_u32 v40, -2, v40\n"
    "v_min_u32 v41, -2, v41\n"
    "v_min_u32 v42, -2, v42\n"
    "v_min_u32 v43, -2, v43\n"
    "buffer_store_dwordx2 v[44:45], v109, s[36:39], s85 offen sc1\n"
    "buffer_store_dwordx4 v[40:43], v108, s[36:39], s85 offen sc1\n"
    "s_setprio 0\n"
    "s_add_u32 m0, s13, 51200\n"
    "s_nop 0\n"
    "buffer_load_dwordx4 v124, s[44:47], s20 offen lds\n"
    "s_add_u32 m0, s13, 55296\n"
    "s_cmp_ge_u32 s4, 1\n"
    "s_cselect_b32 s15, s21, s20\n"
    "buffer_load_dwordx4 v125, s[44:47], s15 offen lds\n"
    "s_add_u32 m0, s13, 59392\n"
    "s_cmp_ge_u32 s4, 2\n"
    "s_cselect_b32 s15, s22, s21\n"
    "buffer_load_dwordx4 v126, s[44:47], s15 offen lds\n"
    "s_waitcnt vmcnt(5)\n"
    "s_barrier\n"
    "s_cmp_eq_u32 s92, 99\n"
    "s_cbranch_scc1 L_wl%=\n"
    "s_add_u32 m0, s13, 63488\n"
    "s_cmp_ge_u32 s4, 3\n"
    "s_cselect_b32 s15, s23, s22\n"
    "buffer_load_dwordx4 v127, s[44:47], s15 offen lds \n"
    "s_add_u32 m0, s13, 67584\n"
    "s_nop 0\n"
    "buffer_load_dwordx4 v7, s[44:47], s23 offen lds \n"
    "s_add_u32 m0, s13, 71680\n"
    "s_nop 0\n"
    "buffer_load_dwordx4 v124, s[44:47], s24 offen lds \n"
    "s_add_u32 m0, s13, 75776\n"
    "s_cmp_ge_u32 s4, 1\n"
    "s_cselect_b32 s15, s25, s24\n"
    "buffer_load_dwordx4 v125, s[44:47], s15 offen lds \n"
    "s_add_u32 m0, s13, 79872\n"
    "s_cmp_ge_u32 s4, 2\n"
    "s_cselect_b32 s15, s26, s25\n"
    "buffer_load_dwordx4 v126, s[44:47], s15 offen lds \n"
    "s_add_u32 m0, s13, 83968\n"
    "s_cmp_ge_u32 s4, 3\n"
    "s_cselect_b32 s15, s27, s26\n"
    "buffer_load_dwordx4 v127, s[44:47], s15 offen lds \n"
    "s_add_u32 m0, s13, 88064\n"
    "s_nop 0\n"
    "buffer_load_dwordx4 v7, s[44:47], s27 offen lds \n"
    "s_add_u32 m0, s13, 92160\n"
    "s_nop 0\n"
    "buffer_load_dwordx4 v124, s[44:47], s40 offen lds \n"
    "L_wl%=:\n"
    "buffer_load_dwordx4 v[40:43], v112, s[36:39], s87 offen sc1\n"
    "buffer_load_dwordx4 v[44:47], v113, s[36:39], s87 offen sc1\n"
    "buffer_load_dwordx4 v[48:51], v114, s[36:39], s87 offen sc1\n"
    "buffer_load_dwordx4 v[52:55], v115, s[36:39], s87 offen sc1\n"
    "buffer_load_dwordx4 v[56:59], v112, s[36:39], s88 offen sc1\n"
    "buffer_load_dwordx4 v[60:63], v113, s[36:39], s88 offen sc1\n"
    "buffer_load_dwordx4 v[64:67], v114, s[36:39], s88 offen sc1\n"
    "buffer_load_dwordx4 v[68:71], v115, s[36:39], s88 offen sc1\n"
    "buffer_load_dwordx4 v[72:75], v112, s[36:39], s89 offen sc1\n"
    "buffer_load_dwordx4 v[76:79], v113, s[36:39], s89 offen sc1\n"
    "buffer_load_dwordx4 v[80:83], v114, s[36:39], s89 offen sc1\n"
    "buffer_load_dwordx4 v[84:87], v115, s[36:39], s89 offen sc1\n"
    "buffer_load_dwordx4 v[88:91], v112, s[36:39], s90 offen sc1\n"
    "buffer_load_dwordx4 v[92:95], v113, s[36:39], s90 offen sc1\n"
    "buffer_load_dwordx4 v[96:99], v114, s[36:39], s90 offen sc1\n"
    "buffer_load_dwordx4 v[100:103], v115, s[36:39], s90 offen sc1\n"
    "s_add_u32 m0, s13, 96256\n"
    "s_cmp_ge_u32 s4, 1\n"
    "s_cselect_b32 s15, s41, s40\n"
    "buffer_load_dwordx4 v125, s[44:47], s15 offen lds\n"
    "s_add_u32 s15, s20, 409600\n"
    "s_add_u32 s16, s13, 100352\n"
    "s_sub_u32 s17, s13, 2048\n"
    "s_cmp_lt_u32 s4, 2\n"
    "s_cselect_b32 s15, s41, s15\n"
    "s_cselect_b32 m0, s16, s17\n"
    "s_nop 0\n"
    "buffer_load_dwordx4 v126, s[44:47], s15 offen lds\n"
    "s_add_u32 m0, s13, 2048\n"
    "s_cmp_ge_u32 s4, 3\n"
    "s_cselect_b32 s15, s21, s20\n"
    "s_add_u32 s15, s15, 409600\n"
    "buffer_load_dwordx4 v127, s[44:47], s15 offen lds\n"
    "s_add_u32 m0, s13, 6144\n"
    "s_add_u32 s15, s21, 409600\n"
    "buffer_load_dwordx4 v7, s[44:47], s15 offen lds\n"
    "s_add_u32 m0, s13, 10240\n"
    "s_add_u32 s15, s22, 409600\n"
    "buffer_load_dwordx4 v124, s[44:47], s15 offen lds\n"
    "s_setprio 2\n"
    "s_waitcnt vmcnt(17)\n"
    "v_max3_u32 v11, v40, v41, v42\n"
    "v_max3_u32 v11, v11, v43, v44\n"
    "v_max3_u32 v11, v11, v45, v46\n"
    "v_max3_u32 v11, v11, v47, v48\n"
    "v_max3_u32 v11, v11, v49, v50\n"
    "v_max3_u32 v11, v11, v51, v52\n"
    "v_max3_u32 v11, v11, v53, v54\n"
    "v_max_u32 v11, v11, v55\n"
    "v_cmp_eq_u32 vcc, -1, v11\n"
    "s_cmp_lg_u64 vcc, 0\n"
    "s_cbranch_scc1 L_g0_slow%=\n"
    "L_g0_done%=:\n"
    "s_waitcnt vmcnt(13)\n"
    "v_max3_u32 v11, v56, v57, v58\n"
    "v_mfma_f32_16x16x32_bf16 v[16:19], v[120:123], v[40:43], 0\n"
    "v_max3_u32 v11, v11, v59, v60\n"
    "v_max3_u32 v11, v11, v61, v62\n"
    "v_mfma_f32_16x16x32_bf16 v[20:23], v[120:123], v[44:47], 0\n"
    "v_max3_u32 v11, v11, v63, v64\n"
    "v_max3_u32 v11, v11, v65, v66\n"
    "v_mfma_f32_16x16x32_bf16 v[24:27], v[120:123], v[48:51], 0\n"
    "v_max3_u32 v11, v11, v67, v68\n"
    "v_max3_u32 v11, v11, v69, v70\n"
    "v_mfma_f32_16x16x32_bf16 v[28:31], v[120:123], v[52:55], 0\n"
    "v_max_u32 v11, v11, v71\n"
    "v_cmp_eq_u32 vcc, -1, v11\n"
    "s_cmp_lg_u64 vcc, 0\n"
    "s_cbranch_scc1 L_g1_slow%=\n"
    "L_g1_done%=:\n"
    "s_waitcnt vmcnt(9)\n"
    "v_max3_u32 v11, v72, v73, v74\n"
    "v_mfma_f32_16x16x32_bf16 v[16:19], v[120:123], v[56:59], v[16:19]\n"
    "v_max3_u32 v11, v11, v75, v76\n"
    "v_max3_u32 v11, v11, v77, v78\n"
    "v_mfma_f32_16x16x32_bf16 v[20:23], v[120:123], v[60:63], v[20:23]\n"
    "v_max3_u32 v11, v11, v79, v80\n"
    "v_max3_u32 v11, v11, v81, v82\n"
    "v_mfma_f32_16x16x32_bf16 v[24:27], v[120:123], v[64:67], v[24:27]\n"
    "v_max3_u32 v11, v11, v83, v84\n"
    "v_max3_u32 v11, v11, v85, v86\n"
    "v_mfma_f32_16x16x32_bf16 v[28:31], v[120:123], v[68:71], v[28:31]\n"
    "v_max_u32 v11, v11, v87\n"
    "v_cmp_eq_u32 vcc, -1, v11\n"
    "s_cmp_lg_u64 vcc, 0\n"
    "s_cbranch_scc1 L_g2_slow%=\n"
    "L_g2_done%=:\n"
    "s_waitcnt vmcnt(5)\n"
    "v_max3_u32 v11, v88, v89, v90\n"
    "v_mfma_f32_16x16x32_bf16 v[16:19], v[120:123], v[72:75], v[16:19]\n"
    "v_max3_u32 v11, v11, v91, v92\n"
    "v_max3_u32 v11, v11, v93, v94\n"
    "v_mfma_f32_16x16x32_bf16 v[20:23], v[120:123], v[76:79], v[20:23]\n"
    "v_max3_u32 v11, v11, v95, v96\n"
    "v_max3_u32 v11, v11, v97, v98\n"
    "v_mfma_f32_16x16x32_bf16 v[24:27], v[120:123], v[80:83], v[24:27]\n"
    "v_max3_u32 v11, v11, v99, v100\n"
    "v_max3_u32 v11, v11, v101, v102\n"
    "v_mfma_f32_16x16x32_bf16 v[28:31], v[120:123], v[84:87], v[28:31]\n"
    "v_max_u32 v11, v11, v103\n"
    "v_cmp_eq_u32 vcc, -1, v11\n"
    "s_cmp_lg_u64 vcc, 0\n"
    "s_cbranch_scc1 L_g3_slow%=\n"
    "L_g3_done%=:\n"
    "v_mfma_f32_16x16x32_bf16 v[16:19], v[120:123], v[88:91], v[16:19]\n"
    "v_mfma_f32_16x16x32_bf16 v[20:23], v[120:123], v[92:95], v[20:23]\n"
    "v_mfma_f32_16x16x32_bf16 v[24:27], v[120:123], v[96:99], v[24:27]\n"
    "v_mfma_f32_16x16x32_bf16 v[28:31], v[120:123], v[100:103], v[28:31]\n"
    "s_nop 4\n"
    "v_cvt_pk_bf16_f32 v40, v16, v17\n"
    "v_cvt_pk_bf16_f32 v41, v18, v19\n"
    "ds_write_b64 v116, v[40:41]\n"
    "v_cvt_pk_bf16_f32 v42, v20, v21\n"
    "v_cvt_pk_bf16_f32 v43, v22, v23\n"
    "ds_write_b64 v117, v[42:43]\n"
    "v_cvt_pk_bf16_f32 v44, v24, v25\n"
    "v_cvt_pk_bf16_f32 v45, v26, v27\n"
    "ds_write_b64 v118, v[44:45]\n"
    "v_cvt_pk_bf16_f32 v46, v28, v29\n"
    "v_cvt_pk_bf16_f32 v47, v30, v31\n"
    "ds_write_b64 v119, v[46:47]\n"
    "s_setprio 0\n"
    "s_waitcnt lgkmcnt(0)\n"
    "s_barrier\n"
    "s_cmp_eq_u32 s92, 99\n"
    "s_cbranch_scc0 L_wh%=\n"
    "s_add_u32 m0, s13, 63488\n"
    "s_cmp_ge_u32 s4, 3\n"
    "s_cselect_b32 s15, s23, s22\n"
    "buffer_load_dwordx4 v127, s[44:47], s15 offen lds \n"
    "s_add_u32 m0, s13, 67584\n"
    "s_nop 0\n"
    "buffer_load_dwordx4 v7, s[44:47], s23 offen lds \n"
    "s_add_u32 m0, s13, 71680\n"
    "s_nop 0\n"
    "buffer_load_dwordx4 v124, s[44:47], s24 offen lds \n"
    "s_add_u32 m0, s13, 75776\n"
    "s_cmp_ge_u32 s4, 1\n"
    "s_cselect_b32 s15, s25, s24\n"
    "buffer_load_dwordx4 v125, s[44:47], s15 offen lds \n"
    "s_add_u32 m0, s13, 79872\n"
    "s_cmp_ge_u32 s4, 2\n"
    "s_cselect_b32 s15, s26, s25\n"
    "buffer_load_dwordx4 v126, s[44:47], s15 offen lds \n"
    "s_add_u32 m0, s13, 83968\n"
    "s_cmp_ge_u32 s4, 3\n"
    "s_cselect_b32 s15, s27, s26\n"
    "buffer_load_dwordx4 v127, s[44:47], s15 offen lds \n"
    "s_add_u32 m0, s13, 88064\n"
    "s_nop 0\n"
    "buffer_load_dwordx4 v7, s[44:47], s27 offen lds \n"
    "s_add_u32 m0, s13, 92160\n"
    "s_nop 0\n"
    "buffer_load_dwordx4 v124, s[44:47], s40 offen lds \n"
    "L_wh%=:\n"
    "s_add_u32 m0, s13, 14336\n"
    "s_cmp_ge_u32 s4, 1\n"
    "s_cselect_b32 s15, s23, s22\n"
    "s_add_u32 s15, s15, 409600\n"
    "buffer_load_dwordx4 v125, s[44:47], s15 offen lds\n"
    "s_add_u32 m0, s13, 18432\n"
    "s_cmp_ge_u32 s4, 2\n"
    "s_cselect_b32 s15, s24, s23\n"
    "s_add_u32 s15, s15, 409600\n"
    "buffer_load_dwordx4 v126, s[44:47], s15 offen lds\n"
    "s_add_u32 m0, s13, 22528\n"
    "s_cmp_ge_u32 s4, 3\n"
    "s_cselect_b32 s15, s25, s24\n"
    "s_add_u32 s15, s15, 409600\n"
    "buffer_load_dwordx4 v127, s[44:47], s15 offen lds\n"
    "s_waitcnt vmcnt(3)\n"
    "s_barrier\n"
    "ds_read_b64_tr_b16 a[0:1], v2 offset:0\n"
    "s_add_u32 m0, s13, 26624\n"
    "s_add_u32 s15, s25, 409600\n"
    "buffer_load_dwordx4 v7, s[44:47], s15 offen lds\n"
    "ds_read_b64_tr_b16 a[2:3], v2 offset:1280\n"
    "ds_read_b64_tr_b16 a[4:5], v2 offset:32\n"
    "ds_read_b64_tr_b16 a[6:7], v2 offset:1312\n"
    "ds_read_b64_tr_b16 a[8:9], v2 offset:64\n"
    "ds_read_b64_tr_b16 a[10:11], v2 offset:1344\n"
    "ds_read_b64_tr_b16 a[12:13], v2 offset:96\n"
    "ds_read_b64_tr_b16 a[14:15], v2 offset:1376\n"
    "ds_read_b64_tr_b16 a[16:17], v2 offset:128\n"
    "s_add_u32 m0, s13, 30720\n"
    "s_add_u32 s15, s26, 409600\n"
    "buffer_load_dwordx4 v124, s[44:47], s15 offen lds\n"
    "ds_read_b64_tr_b16 a[18:19], v2 offset:1408\n"
    "ds_read_b64_tr_b16 a[20:21], v2 offset:5120\n"
    "ds_read_b64_tr_b16 a[22:23], v2 offset:6400\n"
    "ds_read_b64_tr_b16 a[24:25], v2 offset:5152\n"
    "ds_read_b64_tr_b16 a[26:27], v2 offset:6432\n"
    "ds_read_b64_tr_b16 a[28:29], v2 offset:5184\n"
    "ds_read_b64_tr_b16 a[30:31], v2 offset:6464\n"
    "ds_read_b64_tr_b16 a[32:33], v2 offset:5216\n"
    "s_add_u32 m0, s13, 34816\n"
    "s_cmp_ge_u32 s4, 1\n"
    "s_cselect_b32 s15, s27, s26\n"
    "s_add_u32 s15, s15, 409600\n"
    "buffer_load_dwordx4 v125, s[44:47], s15 offen lds\n"
    "ds_read_b64_tr_b16 a[34:35], v2 offset:6496\n"
    "ds_read_b64_tr_b16 a[36:37], v2 offset:5248\n"
    "ds_read_b64_tr_b16 a[38:39], v2 offset:6528\n"
    "ds_read_b64_tr_b16 a[40:41], v2 offset:10240\n"
    "ds_read_b64_tr_b16 a[42:43], v2 offset:11520\n"
    "ds_read_b64_tr_b16 a[44:45], v2 offset:10272\n"
    "ds_read_b64_tr_b16 a[46:47], v2 offset:11552\n"
    "ds_read_b64_tr_b16 a[48:49], v2 offset:10304\n"
    "s_add_u32 m0, s13, 38912\n"
    "s_cmp_ge_u32 s4, 2\n"
    "s_cselect_b32 s15, s40, s27\n"
    "s_add_u32 s15, s15, 409600\n"
    "buffer_load_dwordx4 v126, s[44:47], s15 offen lds\n"
    "ds_read_b64_tr_b16 a[50:51], v2 offset:11584\n"
    "ds_read_b64_tr_b16 a[52:53], v2 offset:10336\n"
    "ds_read_b64_tr_b16 a[54:55], v2 offset:11616\n"
    "ds_read_b64_tr_b16 a[56:57], v2 offset:10368\n"
    "ds_read_b64_tr_b16 a[58:59], v2 offset:11648\n"
    "ds_read_b64_tr_b16 a[60:61], v2 offset:15360\n"
    "ds_read_b64_tr_b16 a[62:63], v2 offset:16640\n"
    "ds_read_b64_tr_b16 a[64:65], v2 offset:15392\n"
    "s_add_u32 m0, s13, 43008\n"
    "s_cmp_ge_u32 s4, 3\n"
    "s_cselect_b32 s15, s41, s40\n"
    "s_add_u32 s15, s15, 409600\n"
    "buffer_load_dwordx4 v127, s[44:47], s15 offen lds\n"
    "ds_read_b64_tr_b16 a[66:67], v2 offset:16672\n"
    "ds_read_b64_tr_b16 a[68:69], v2 offset:15424\n"
    "ds_read_b64_tr_b16 a[70:71], v2 offset:16704\n"
    "ds_read_b64_tr_b16 a[72:73], v2 offset:15456\n"
    "ds_read_b64_tr_b16 a[74:75], v2 offset:16736\n"
    "ds_read_b64_tr_b16 a[76:77], v2 offset:15488\n"
    "ds_read_b64_tr_b16 a[78:79], v2 offset:16768\n"
    "ds_read_b64_tr_b16 a[80:81], v2 offset:20480\n"
    "s_add_u32 m0, s13, 47104\n"
    "s_add_u32 s15, s41, 409600\n"
    "buffer_load_dwordx4 v7, s[44:47], s15 offen lds\n"
    "ds_read_b64_tr_b16 a[82:83], v2 offset:21760\n"
    "ds_read_b64_tr_b16 a[84:85], v2 offset:20512\n"
    "ds_read_b64_tr_b16 a[86:87], v2 offset:21792\n"
    "ds_read_b64_tr_b16 a[88:89], v2 offset:20544\n"
    "ds_read_b64_tr_b16 a[90:91], v2 offset:21824\n"
    "ds_read_b64_tr_b16 a[92:93], v2 offset:20576\n"
    "ds_read_b64_tr_b16 a[94:95], v2 offset:21856\n"
    "ds_read_b64_tr_b16 a[96:97], v2 offset:20608\n"
    "ds_read_b64_tr_b16 a[98:99], v2 offset:21888\n"
    "ds_read_b64_tr_b16 a[100:101], v2 offset:25600\n"
    "ds_read_b64_tr_b16 a[102:103], v2 offset:26880\n"
    "ds_read_b64_tr_b16 a[104:105], v2 offset:25632\n"
    "ds_read_b64_tr_b16 a[106:107], v2 offset:26912\n"
    "ds_read_b64_tr_b16 a[108:109], v2 offset:25664\n"
    "ds_read_b64_tr_b16 a[110:111], v2 offset:26944\n"
    "ds_read_b64_tr_b16 a[112:113], v2 offset:25696\n"
    "ds_read_b64_tr_b16 a[114:115], v2 offset:26976\n"
    "ds_read_b64_tr_b16 a[116:117], v2 offset:25728\n"
    "ds_read_b64_tr_b16 a[118:119], v2 offset:27008\n"
    "ds_read_b64_tr_b16 a[120:121], v2 offset:30720\n"
    "ds_read_b64_tr_b16 a[122:123], v2 offset:32000\n"
    "ds_read_b64_tr_b16 a[124:125], v2 offset:30752\n"
    "ds_read_b64_tr_b16 a[126:127], v2 offset:32032\n"
    "ds_read_b64_tr_b16 v[80:81], v2 offset:30784\n"
    "ds_read_b64_tr_b16 v[82:83], v2 offset:32064\n"
    "ds_read_b64_tr_b16 v[84:85], v2 offset:30816\n"
    "ds_read_b64_tr_b16 v[86:87], v2 offset:32096\n"
    "ds_read_b64_tr_b16 v[88:89], v2 offset:30848\n"
    "ds_read_b64_tr_b16 v[90:91], v2 offset:32128\n"
    "ds_read_b64_tr_b16 v[92:93], v2 offset:35840\n"
    "ds_read_b64_tr_b16 v[94:95], v2 offset:37120\n"
    "ds_read_b64_tr_b16 v[96:97], v2 offset:35872\n"
    "ds_read_b64_tr_b16 v[98:99], v2 offset:37152\n"
    "ds_read_b64_tr_b16 v[100:101], v2 offset:35904\n"
    "ds_read_b64_tr_b16 v[102:103], v2 offset:37184\n"
    "ds_read_b64_tr_b16 v[104:105], v2 offset:35936\n"
    "ds_read_b64_tr_b16 v[106:107], v2 offset:37216\n"
    "ds_read_b64_tr_b16 v[108:109], v2 offset:35968\n"
    "ds_read_b64_tr_b16 v[110:111], v2 offset:37248\n"
    "ds_read_b64_tr_b16 v[112:113], v2 offset:40960\n"
    "ds_read_b64_tr_b16 v[114:115], v2 offset:42240\n"
    "ds_read_b64_tr_b16 v[116:117], v2 offset:40992\n"
    "ds_read_b64_tr_b16 v[118:119], v2 offset:42272\n"
    "ds_read_b64_tr_b16 v[120:121], v2 offset:41024\n"
    "ds_read_b64_tr_b16 v[122:123], v2 offset:42304\n"
    "ds_read_b64_tr_b16 v[124:125], v2 offset:41056\n"
    "ds_read_b64_tr_b16 v[126:127], v2 offset:42336\n"
    "s_waitcnt vmcnt(0)\n"
    "s_barrier\n"
    "s_barrier\n"
    "s_branch L_G2%=\n"
    "L_B%=:\n"
    "v_lshrrev_b32 v11, 1, v1\n"
    "v_and_b32 v11, 30, v11\n"
    "v_mul_u32_u24 v11, 80, v11\n"
    "v_and_b32 v12, 3, v1\n"
    "v_and_b32 v13, 2, v1\n"
    "v_lshl_add_u32 v9, v13, 1, v12\n"
    "v_lshl_add_u32 v9, v9, 3, v11\n"
    "v_lshl_add_u32 v10, v13, 2, v11\n"
    "v_add_u32 v10, 32, v10\n"
    "v_and_b32 v11, 15, v1\n"
    "v_bfe_u32 v13, v1, 1, 3\n"
    "v_lshrrev_b32 v12, 4, v1\n"
    "v_xor_b32 v13, v13, v12\n"
    "v_lshlrev_b32 v13, 4, v13\n"
    "v_lshl_or_b32 v13, v11, 7, v13\n"
    "s_add_u32 s15, s84, 86016\n"
    "v_add_u32 v7, s15, v13\n"
    "v_xor_b32 v8, 64, v7\n"
    "s_mov_b32 s38, 3522560\n"
    "s_mov_b32 s39, 0x20000\n"
    "s_waitcnt vmcnt(4)\n"
    "s_barrier\n"
    "ds_read_b128 a[0:3], v7 offset:16384\n"
    "ds_read_b128 a[4:7], v7 offset:18432\n"
    "ds_read_b64_tr_b16 v[84:85], v9 offset:0\n"
    "ds_read_b64_tr_b16 v[86:87], v9 offset:80\n"
    "ds_read_b64_tr_b16 v[88:89], v9 offset:16\n"
    "ds_read_b64_tr_b16 v[90:91], v9 offset:96\n"
    "ds_read_b64_tr_b16 v[92:93], v10 offset:0\n"
    "ds_read_b64_tr_b16 v[94:95], v10 offset:80\n"
    "ds_read_b128 a[8:11], v8 offset:16384\n"
    "ds_read_b128 a[12:15], v8 offset:18432\n"
    "s_waitcnt lgkmcnt(6)\n"
    "v_mfma_f32_16x16x32_bf16 v[16:19], v[84:87], a[0:3], 0\n"
    "ds_read_b64_tr_b16 v[96:97], v9 offset:2560\n"
    "v_mfma_f32_16x16x32_bf16 v[20:23], v[84:87], a[4:7], 0\n"
    "ds_read_b64_tr_b16 v[98:99], v9 offset:2640\n"
    "s_waitcnt lgkmcnt(6)\n"
    "v_mfma_f32_16x16x32_bf16 v[24:27], v[88:91], a[0:3], 0\n"
    "ds_read_b64_tr_b16 v[100:101], v9 offset:2576\n"
    "v_mfma_f32_16x16x32_bf16 v[28:31], v[88:91], a[4:7], 0\n"
    "ds_read_b64_tr_b16 v[102:103], v9 offset:2656\n"
    "s_waitcnt lgkmcnt(6)\n"
    "v_mfma_f32_16x16x32_bf16 v[32:35], v[92:95], a[0:3], 0\n"
    "ds_read_b64_tr_b16 v[104:105], v10 offset:2560\n"
    "v_mfma_f32_16x16x32_bf16 v[36:39], v[92:95], a[4:7], 0\n"
    "ds_read_b64_tr_b16 v[106:107], v10 offset:2640\n"
    "s_waitcnt vmcnt(0)\n"
    "s_barrier\n"
    "ds_read_b128 a[16:19], v7 offset:32768\n"
    "ds_read_b128 a[20:23], v7 offset:34816\n"
    "ds_read_b64_tr_b16 v[108:109], v9 offset:5120\n"
    "ds_read_b64_tr_b16 v[110:111], v9 offset:5200\n"
    "ds_read_b64_tr_b16 v[112:113], v9 offset:5136\n"
    "ds_read_b64_tr_b16 v[114:115], v9 offset:5216\n"
    "ds_read_b64_tr_b16 v[116:117], v10 offset:5120\n"
    "ds_read_b64_tr_b16 v[118:119], v10 offset:5200\n"
    "ds_read_b128 a[24:27], v8 offset:32768\n"
    "ds_read_b128 a[28:31], v8 offset:34816\n"
    "s_waitcnt lgkmcnt(14)\n"
    "v_mfma_f32_16x16x32_bf16 v[16:19], v[96:99], a[8:11], v[16:19]\n"
    "v_mfma_f32_16x16x32_bf16 v[20:23], v[96:99], a[12:15], v[20:23]\n"
    "s_waitcnt lgkmcnt(12)\n"
    "v_mfma_f32_16x16x32_bf16 v[24:27], v[100:103], a[8:11], v[24:27]\n"
    "v_mfma_f32_16x16x32_bf16 v[28:31], v[100:103], a[12:15], v[28:31]\n"
    "s_waitcnt lgkmcnt(10)\n"
    "v_mfma_f32_16x16x32_bf16 v[32:35], v[104:107], a[8:11], v[32:35]\n"
    "v_mfma_f32_16x16x32_bf16 v[36:39], v[104:107], a[12:15], v[36:39]\n"
    "s_waitcnt lgkmcnt(6)\n"
    "v_mfma_f32_16x16x32_bf16 v[16:19], v[108:111], a[16:19], v[16:19]\n"
    "ds_read_b64_tr_b16 v[84:85], v9 offset:7680\n"
    "v_mfma_f32_16x16x32_bf16 v[20:23], v[108:111], a[20:23], v[20:23]\n"
    "ds_read_b64_tr_b16 v[86:87], v9 offset:7760\n"
    "s_waitcnt lgkmcnt(6)\n"
    "v_mfma_f32_16x16x32_bf16 v[24:27], v[112:115], a[16:19], v[24:27]\n"
    "ds_read_b64_tr_b16 v[88:89], v9 offset:7696\n"
    "v_mfma_f32_16x16x32_bf16 v[28:31], v[112:115], a[20:23], v[28:31]\n"
    "ds_read_b64_tr_b16 v[90:91], v9 offset:7776\n"
    "s_waitcnt lgkmcnt(6)\n"
    "v_mfma_f32_16x16x32_bf16 v[32:35], v[116:119], a[16:19], v[32:35]\n"
    "ds_read_b64_tr_b16 v[92:93], v10 offset:7680\n"
    "v_mfma_f32_16x16x32_bf16 v[36:39], v[116:119], a[20:23], v[36:39]\n"
    "ds_read_b64_tr_b16 v[94:95], v10 offset:7760\n"
    "s_xor_b32 s9, s8, 1\n"
    "v_lshlrev_b32 v14, 4, v1\n"
    "s_lshl_b32 s15, s7, 2\n"
    "s_add_u32 s15, s15, s4\n"
    "s_mul_i32 s15, s15, 160\n"
    "s_mul_i32 s16, s9, 5\n"
    "s_add_u32 s15, s15, s16\n"
    "s_lshl_b32 s15, s15, 8\n"
    "s_add_u32 s86, s15, 901120\n"
    "s_barrier\n"
    "ds_read_b128 a[32:35], v7 offset:49152\n"
    "ds_read_b128 a[36:39], v7 offset:51200\n"
    "ds_read_b64_tr_b16 v[96:97], v9 offset:10240\n"
    "ds_read_b64_tr_b16 v[98:99], v9 offset:10320\n"
    "ds_read_b64_tr_b16 v[100:101], v9 offset:10256\n"
    "ds_read_b64_tr_b16 v[102:103], v9 offset:10336\n"
    "ds_read_b64_tr_b16 v[104:105], v10 offset:10240\n"
    "ds_read_b64_tr_b16 v[106:107], v10 offset:10320\n"
    "ds_read_b128 a[40:43], v8 offset:49152\n"
    "ds_read_b128 a[44:47], v8 offset:51200\n"
    "s_waitcnt lgkmcnt(14)\n"
    "v_mfma_f32_16x16x32_bf16 v[16:19], v[84:87], a[24:27], v[16:19]\n"
    "v_mfma_f32_16x16x32_bf16 v[20:23], v[84:87], a[28:31], v[20:23]\n"
    "s_waitcnt lgkmcnt(12)\n"
    "v_mfma_f32_16x16x32_bf16 v[24:27], v[88:91], a[24:27], v[24:27]\n"
    "v_mfma_f32_16x16x32_bf16 v[28:31], v[88:91], a[28:31], v[28:31]\n"
    "s_waitcnt lgkmcnt(10)\n"
    "v_mfma_f32_16x16x32_bf16 v[32:35], v[92:95], a[24:27], v[32:35]\n"
    "v_mfma_f32_16x16x32_bf16 v[36:39], v[92:95], a[28:31], v[36:39]\n"
    "s_waitcnt lgkmcnt(6)\n"
    "v_mfma_f32_16x16x32_bf16 v[16:19], v[96:99], a[32:35], v[16:19]\n"
    "ds_read_b64_tr_b16 v[108:109], v9 offset:12800\n"
    "v_mfma_f32_16x16x32_bf16 v[20:23], v[96:99], a[36:39], v[20:23]\n"
    "ds_read_b64_tr_b16 v[110:111], v9 offset:12880\n"
    "s_waitcnt lgkmcnt(6)\n"
    "v_mfma_f32_16x16x32_bf16 v[24:27], v[100:103], a[32:35], v[24:27]\n"
    "ds_read_b64_tr_b16 v[112:113], v9 offset:12816\n"
    "v_mfma_f32_16x16x32_bf16 v[28:31], v[100:103], a[36:39], v[28:31]\n"
    "ds_read_b64_tr_b16 v[114:115], v9 offset:12896\n"
    "s_waitcnt lgkmcnt(6)\n"
    "v_mfma_f32_16x16x32_bf16 v[32:35], v[104:107], a[32:35], v[32:35]\n"
    "ds_read_b64_tr_b16 v[116:117], v10 offset:12800\n"
    "v_mfma_f32_16x16x32_bf16 v[36:39], v[104:107], a[36:39], v[36:39]\n"
    "ds_read_b64_tr_b16 v[118:119], v10 offset:12880\n"
    "v_lshrrev_b32 v11, 5, v1\n"
    "v_and_b32 v12, 1, v1\n"
    "v_cmp_eq_u32 s[80:81], v11, v12\n"
    "v_and_b32 v12, 18, v1\n"
    "v_cmp_eq_u32 vcc, 0, v12\n"
    "v_bfe_u32 v13, v1, 2, 2\n"
    "v_mul_u32_u24 v13, 40, v13\n"
    "s_and_b64 s[82:83], vcc, s[80:81]\n"
    "s_and_b32 s15, s9, 1\n"
    "s_mul_i32 s15, s15, 1600\n"
    "s_add_u32 s15, s15, 155680\n"
    "v_add_u32 v13, s15, v13\n"
    "v_mul_u32_u24 v12, 960, v11\n"
    "v_add_u32 v126, v12, v13\n"
    "v_mul_u32_u24 v12, 160, v11\n"
    "v_add_u32 v127, v12, v13\n"
    "v_add_u32 v127, 640, v127\n"
    "s_barrier\n"
    "ds_read_b128 a[48:51], v7 offset:0\n"
    "ds_read_b128 a[52:55], v7 offset:2048\n"
    "ds_read_b64_tr_b16 v[84:85], v9 offset:15360\n"
    "ds_read_b64_tr_b16 v[86:87], v9 offset:15440\n"
    "ds_read_b64_tr_b16 v[88:89], v9 offset:15376\n"
    "ds_read_b64_tr_b16 v[90:91], v9 offset:15456\n"
    "ds_read_b64_tr_b16 v[92:93], v10 offset:15360\n"
    "ds_read_b64_tr_b16 v[94:95], v10 offset:15440\n"
    "ds_read_b128 a[56:59], v8 offset:0\n"
    "ds_read_b128 a[60:63], v8 offset:2048\n"
    "s_waitcnt lgkmcnt(14)\n"
    "v_mfma_f32_16x16x32_bf16 v[16:19], v[108:111], a[40:43], v[16:19]\n"
    "v_mfma_f32_16x16x32_bf16 v[20:23], v[108:111], a[44:47], v[20:23]\n"
    "s_waitcnt lgkmcnt(12)\n"
    "v_mfma_f32_16x16x32_bf16 v[24:27], v[112:115], a[40:43], v[24:27]\n"
    "v_mfma_f32_16x16x32_bf16 v[28:31], v[112:115], a[44:47], v[28:31]\n"
    "s_waitcnt lgkmcnt(10)\n"
    "v_mfma_f32_16x16x32_bf16 v[32:35], v[116:119], a[40:43], v[32:35]\n"
    "v_mfma_f32_16x16x32_bf16 v[36:39], v[116:119], a[44:47], v[36:39]\n"
    "s_waitcnt lgkmcnt(6)\n"
    "v_mfma_f32_16x16x32_bf16 v[16:19], v[84:87], a[48:51], v[16:19]\n"
    "ds_read_b64_tr_b16 v[96:97], v9 offset:17920\n"
    "v_mfma_f32_16x16x32_bf16 v[20:23], v[84:87], a[52:55], v[20:23]\n"
    "ds_read_b64_tr_b16 v[98:99], v9 offset:18000\n"
    "s_waitcnt lgkmcnt(6)\n"
    "v_mfma_f32_16x16x32_bf16 v[24:27], v[88:91], a[48:51], v[24:27]\n"
    "ds_read_b64_tr_b16 v[100:101], v9 offset:17936\n"
    "v_mfma_f32_16x16x32_bf16 v[28:31], v[88:91], a[52:55], v[28:31]\n"
    "ds_read_b64_tr_b16 v[102:103], v9 offset:18016\n"
    "s_waitcnt lgkmcnt(6)\n"
    "v_mfma_f32_16x16x32_bf16 v[32:35], v[92:95], a[48:51], v[32:35]\n"
    "ds_read_b64_tr_b16 v[104:105], v10 offset:17920\n"
    "v_mfma_f32_16x16x32_bf16 v[36:39], v[92:95], a[52:55], v[36:39]\n"
    "ds_read_b64_tr_b16 v[106:107], v10 offset:18000\n"
    "v_bfe_u32 v11, v1, 4, 1\n"
    "v_and_b32 v12, 2, v1\n"
    "v_lshlrev_b32 v11, 4, v11\n"
    "v_lshl_add_u32 v11, v12, 2, v11\n"
    "v_subrev_u32 v11, 32, v11\n"
    "v_add_u32 v124, v126, v11\n"
    "v_add_u32 v125, v127, v11\n"
    "v_mov_b32 v11, 158848\n"
    "s_barrier\n"
    "ds_read_b128 a[64:67], v7 offset:16384\n"
    "ds_read_b128 a[68:71], v7 offset:18432\n"
    "ds_read_b64_tr_b16 v[108:109], v9 offset:20480\n"
    "ds_read_b64_tr_b16 v[110:111], v9 offset:20560\n"
    "ds_read_b64_tr_b16 v[112:113], v9 offset:20496\n"
    "ds_read_b64_tr_b16 v[114:115], v9 offset:20576\n"
    "ds_read_b64_tr_b16 v[116:117], v10 offset:20480\n"
    "ds_read_b64_tr_b16 v[118:119], v10 offset:20560\n"
    "ds_read_b128 a[72:75], v8 offset:16384\n"
    "ds_read_b128 a[76:79], v8 offset:18432\n"
    "s_waitcnt lgkmcnt(14)\n"
    "v_mfma_f32_16x16x32_bf16 v[16:19], v[96:99], a[56:59], v[16:19]\n"
    "v_mfma_f32_16x16x32_bf16 v[20:23], v[96:99], a[60:63], v[20:23]\n"
    "s_waitcnt lgkmcnt(12)\n"
    "v_mfma_f32_16x16x32_bf16 v[24:27], v[100:103], a[56:59], v[24:27]\n"
    "v_mfma_f32_16x16x32_bf16 v[28:31], v[100:103], a[60:63], v[28:31]\n"
    "s_waitcnt lgkmcnt(10)\n"
    "v_mfma_f32_16x16x32_bf16 v[32:35], v[104:107], a[56:59], v[32:35]\n"
    "v_mfma_f32_16x16x32_bf16 v[36:39], v[104:107], a[60:63], v[36:39]\n"
    "s_waitcnt lgkmcnt(6)\n"
    "v_mfma_f32_16x16x32_bf16 v[16:19], v[108:111], a[64:67], v[16:19]\n"
    "ds_read_b64_tr_b16 v[84:85], v9 offset:23040\n"
    "v_mfma_f32_16x16x32_bf16 v[20:23], v[108:111], a[68:71], v[20:23]\n"
    "ds_read_b64_tr_b16 v[86:87], v9 offset:23120\n"
    "s_waitcnt lgkmcnt(6)\n"
    "v_mfma_f32_16x16x32_bf16 v[24:27], v[112:115], a[64:67], v[24:27]\n"
    "ds_read_b64_tr_b16 v[88:89], v9 offset:23056\n"
    "v_mfma_f32_16x16x32_bf16 v[28:31], v[112:115], a[68:71], v[28:31]\n"
    "ds_read_b64_tr_b16 v[90:91], v9 offset:23136\n"
    "s_waitcnt lgkmcnt(6)\n"
    "v_mfma_f32_16x16x32_bf16 v[32:35], v[116:119], a[64:67], v[32:35]\n"
    "ds_read_b64_tr_b16 v[92:93], v10 offset:23040\n"
    "v_mfma_f32_16x16x32_bf16 v[36:39], v[116:119], a[68:71], v[36:39]\n"
    "ds_read_b64_tr_b16 v[94:95], v10 offset:23120\n"
    "s_and_b32 s15, s9, 1\n"
    "s_mul_i32 s15, s15, -3200\n"
    "s_add_u32 s15, s15, 1600\n"
    "v_add_u32 v2, s15, v124\n"
    "v_add_u32 v3, s15, v125\n"
    "v_add_u32 v4, s15, v126\n"
    "v_add_u32 v5, s15, v127\n"
    "v_cndmask_b32 v124, v11, v124, s[80:81]\n"
    "v_cndmask_b32 v125, v11, v125, s[80:81]\n"
    "v_cndmask_b32 v126, v11, v126, s[82:83]\n"
    "v_cndmask_b32 v127, v11, v127, s[82:83]\n"
    "v_cndmask_b32 v2, v11, v2, s[80:81]\n"
    "v_cndmask_b32 v3, v11, v3, s[80:81]\n"
    "v_cndmask_b32 v4, v11, v4, s[82:83]\n"
    "v_cndmask_b32 v5, v11, v5, s[82:83]\n"
    "s_mov_b32 s78, 0x3fb8aa3b\n"
    "s_barrier\n"
    "ds_read_b128 a[80:83], v7 offset:32768\n"
    "ds_read_b128 a[84:87], v7 offset:34816\n"
    "ds_read_b64_tr_b16 v[96:97], v9 offset:25600\n"
    "ds_read_b64_tr_b16 v[98:99], v9 offset:25680\n"
    "ds_read_b64_tr_b16 v[100:101], v9 offset:25616\n"
    "ds_read_b64_tr_b16 v[102:103], v9 offset:25696\n"
    "ds_read_b64_tr_b16 v[104:105], v10 offset:25600\n"
    "ds_read_b64_tr_b16 v[106:107], v10 offset:25680\n"
    "ds_read_b128 a[88:91], v8 offset:32768\n"
    "ds_read_b128 a[92:95], v8 offset:34816\n"
    "s_waitcnt lgkmcnt(14)\n"
    "v_mfma_f32_16x16x32_bf16 v[16:19], v[84:87], a[72:75], v[16:19]\n"
    "v_mfma_f32_16x16x32_bf16 v[20:23], v[84:87], a[76:79], v[20:23]\n"
    "s_waitcnt lgkmcnt(12)\n"
    "v_mfma_f32_16x16x32_bf16 v[24:27], v[88:91], a[72:75], v[24:27]\n"
    "v_mfma_f32_16x16x32_bf16 v[28:31], v[88:91], a[76:79], v[28:31]\n"
    "s_waitcnt lgkmcnt(10)\n"
    "v_mfma_f32_16x16x32_bf16 v[32:35], v[92:95], a[72:75], v[32:35]\n"
    "v_mfma_f32_16x16x32_bf16 v[36:39], v[92:95], a[76:79], v[36:39]\n"
    "s_waitcnt lgkmcnt(6)\n"
    "v_mfma_f32_16x16x32_bf16 v[16:19], v[96:99], a[80:83], v[16:19]\n"
    "ds_read_b64_tr_b16 v[108:109], v9 offset:28160\n"
    "v_mfma_f32_16x16x32_bf16 v[20:23], v[96:99], a[84:87], v[20:23]\n"
    "ds_read_b64_tr_b16 v[110:111], v9 offset:28240\n"
    "s_waitcnt lgkmcnt(6)\n"
    "v_mfma_f32_16x16x32_bf16 v[24:27], v[100:103], a[80:83], v[24:27]\n"
    "ds_read_b64_tr_b16 v[112:113], v9 offset:28176\n"
    "v_mfma_f32_16x16x32_bf16 v[28:31], v[100:103], a[84:87], v[28:31]\n"
    "ds_read_b64_tr_b16 v[114:115], v9 offset:28256\n"
    "s_waitcnt lgkmcnt(6)\n"
    "v_mfma_f32_16x16x32_bf16 v[32:35], v[104:107], a[80:83], v[32:35]\n"
    "ds_read_b64_tr_b16 v[116:117], v10 offset:28160\n"
    "v_mfma_f32_16x16x32_bf16 v[36:39], v[104:107], a[84:87], v[36:39]\n"
    "ds_read_b64_tr_b16 v[118:119], v10 offset:28240\n"
    "v_lshrrev_b32 v11, 5, v1\n"
    "v_bfe_u32 v12, v1, 2, 1\n"
    "v_cmp_eq_u32 s[80:81], v11, v12\n"
    "v_bfe_u32 v12, v1, 3, 2\n"
    "v_and_b32 v13, 3, v1\n"
    "v_lshl_or_b32 v12, v12, 2, v13\n"
    "v_cmp_gt_u32 vcc, 5, v12\n"
    "v_mad_u32_u24 v12, v11, 21, v12\n"
    "v_mul_u32_u24 v12, 80, v12\n"
    "s_and_b64 s[82:83], vcc, s[80:81]\n"
    "v_add_u32 v121, 159744, v12\n"
    "v_add_u32 v122, 161024, v12\n"
    "v_mov_b32 v13, 158848\n"
    "v_cndmask_b32 v121, v13, v121, s[80:81]\n"
    "v_cndmask_b32 v122, v13, v122, s[82:83]\n"
    "s_barrier\n"
    "ds_read_b128 a[96:99], v7 offset:49152\n"
    "ds_read_b128 a[100:103], v7 offset:51200\n"
    "ds_read_b64_tr_b16 v[84:85], v9 offset:30720\n"
    "ds_read_b64_tr_b16 v[86:87], v9 offset:30800\n"
    "ds_read_b64_tr_b16 v[88:89], v9 offset:30736\n"
    "ds_read_b64_tr_b16 v[90:91], v9 offset:30816\n"
    "ds_read_b64_tr_b16 v[92:93], v10 offset:30720\n"
    "ds_read_b64_tr_b16 v[94:95], v10 offset:30800\n"
    "ds_read_b128 a[104:107], v8 offset:49152\n"
    "ds_read_b128 a[108:111], v8 offset:51200\n"
    "s_waitcnt lgkmcnt(14)\n"
    "v_mfma_f32_16x16x32_bf16 v[16:19], v[108:111], a[88:91], v[16:19]\n"
    "v_mfma_f32_16x16x32_bf16 v[20:23], v[108:111], a[92:95], v[20:23]\n"
    "s_waitcnt lgkmcnt(12)\n"
    "v_mfma_f32_16x16x32_bf16 v[24:27], v[112:115], a[88:91], v[24:27]\n"
    "v_mfma_f32_16x16x32_bf16 v[28:31], v[112:115], a[92:95], v[28:31]\n"
    "s_waitcnt lgkmcnt(10)\n"
    "v_mfma_f32_16x16x32_bf16 v[32:35], v[116:119], a[88:91], v[32:35]\n"
    "v_mfma_f32_16x16x32_bf16 v[36:39], v[116:119], a[92:95], v[36:39]\n"
    "s_waitcnt lgkmcnt(6)\n"
    "v_mfma_f32_16x16x32_bf16 v[16:19], v[84:87], a[96:99], v[16:19]\n"
    "ds_read_b64_tr_b16 v[96:97], v9 offset:33280\n"
    "v_mfma_f32_16x16x32_bf16 v[20:23], v[84:87], a[100:103], v[20:23]\n"
    "ds_read_b64_tr_b16 v[98:99], v9 offset:33360\n"
    "s_waitcnt lgkmcnt(6)\n"
    "v_mfma_f32_16x16x32_bf16 v[24:27], v[88:91], a[96:99], v[24:27]\n"
    "ds_read_b64_tr_b16 v[100:101], v9 offset:33296\n"
    "v_mfma_f32_16x16x32_bf16 v[28:31], v[88:91], a[100:103], v[28:31]\n"
    "ds_read_b64_tr_b16 v[102:103], v9 offset:33376\n"
    "s_waitcnt lgkmcnt(6)\n"
    "v_mfma_f32_16x16x32_bf16 v[32:35], v[92:95], a[96:99], v[32:35]\n"
    "ds_read_b64_tr_b16 v[104:105], v10 offset:33280\n"
    "v_mfma_f32_16x16x32_bf16 v[36:39], v[92:95], a[100:103], v[36:39]\n"
    "ds_read_b64_tr_b16 v[106:107], v10 offset:33360\n"
    "v_lshrrev_b32 v11, 5, v1\n"
    "v_bfe_u32 v12, v1, 4, 1\n"
    "v_mul_u32_u24 v12, 20480, v12\n"
    "v_and_b32 v13, 15, v1\n"
    "v_lshl_add_u32 v12, v13, 4, v12\n"
    "v_lshl_add_u32 v6, v11, 3, v12\n"
    "v_mul_u32_u24 v13, 768, v11\n"
    "v_add_u32 v123, v13, v12\n"
    "v_and_b32 v12, 31, v1\n"
    "v_lshl_or_b32 v11, v11, 7, v12\n"
    "s_lshl_b32 s15, s4, 5\n"
    "v_add_u32 v11, s15, v11\n"
    "v_mul_u32_u24 v120, 48, v11\n"
    "v_add_u32 v120, 20480, v120\n"
    "s_barrier\n"
    "ds_read_b128 a[112:115], v7 offset:0\n"
    "ds_read_b128 a[116:119], v7 offset:2048\n"
    "ds_read_b64_tr_b16 v[108:109], v9 offset:35840\n"
    "ds_read_b64_tr_b16 v[110:111], v9 offset:35920\n"
    "ds_read_b64_tr_b16 v[112:113], v9 offset:35856\n"
    "ds_read_b64_tr_b16 v[114:115], v9 offset:35936\n"
    "ds_read_b64_tr_b16 v[116:117], v10 offset:35840\n"
    "ds_read_b64_tr_b16 v[118:119], v10 offset:35920\n"
    "ds_read_b128 a[120:123], v8 offset:0\n"
    "ds_read_b128 a[124:127], v8 offset:2048\n"
    "s_waitcnt lgkmcnt(14)\n"
    "v_mfma_f32_16x16x32_bf16 v[16:19], v[96:99], a[104:107], v[16:19]\n"
    "v_mfma_f32_16x16x32_bf16 v[20:23], v[96:99], a[108:111], v[20:23]\n"
    "s_waitcnt lgkmcnt(12)\n"
    "v_mfma_f32_16x16x32_bf16 v[24:27], v[100:103], a[104:107], v[24:27]\n"
    "v_mfma_f32_16x16x32_bf16 v[28:31], v[100:103], a[108:111], v[28:31]\n"
    "s_waitcnt lgkmcnt(10)\n"
    "v_mfma_f32_16x16x32_bf16 v[32:35], v[104:107], a[104:107], v[32:35]\n"
    "v_mfma_f32_16x16x32_bf16 v[36:39], v[104:107], a[108:111], v[36:39]\n"
    "s_waitcnt lgkmcnt(6)\n"
    "v_mfma_f32_16x16x32_bf16 v[16:19], v[108:111], a[112:115], v[16:19]\n"
    "ds_read_b64_tr_b16 v[84:85], v9 offset:38400\n"
    "v_mfma_f32_16x16x32_bf16 v[20:23], v[108:111], a[116:119], v[20:23]\n"
    "ds_read_b64_tr_b16 v[86:87], v9 offset:38480\n"
    "s_waitcnt lgkmcnt(6)\n"
    "v_mfma_f32_16x16x32_bf16 v[24:27], v[112:115], a[112:115], v[24:27]\n"
    "ds_read_b64_tr_b16 v[88:89], v9 offset:38416\n"
    "v_mfma_f32_16x16x32_bf16 v[28:31], v[112:115], a[116:119], v[28:31]\n"
    "ds_read_b64_tr_b16 v[90:91], v9 offset:38496\n"
    "s_waitcnt lgkmcnt(6)\n"
    "v_mfma_f32_16x16x32_bf16 v[32:35], v[116:119], a[112:115], v[32:35]\n"
    "ds_read_b64_tr_b16 v[92:93], v10 offset:38400\n"
    "v_mfma_f32_16x16x32_bf16 v[36:39], v[116:119], a[116:119], v[36:39]\n"
    "ds_read_b64_tr_b16 v[94:95], v10 offset:38480\n"
    "v_lshrrev_b32 v13, 5, v1\n"
    "v_and_b32 v12, 31, v1\n"
    "v_lshl_or_b32 v11, v13, 7, v12\n"
    "s_lshl_b32 s15, s4, 5\n"
    "v_add_u32 v11, s15, v11\n"
    "v_mul_u32_u24 v15, 80, v11\n"
    "s_barrier\n"
    "ds_read_b128 v[52:55], v7 offset:16384\n"
    "ds_read_b128 v[56:59], v7 offset:18432\n"
    "ds_read_b64_tr_b16 v[96:97], v9 offset:40960\n"
    "ds_read_b64_tr_b16 v[98:99], v9 offset:41040\n"
    "ds_read_b64_tr_b16 v[100:101], v9 offset:40976\n"
    "ds_read_b64_tr_b16 v[102:103], v9 offset:41056\n"
    "ds_read_b64_tr_b16 v[104:105], v10 offset:40960\n"
    "ds_read_b64_tr_b16 v[106:107], v10 offset:41040\n"
    "ds_read_b128 v[60:63], v8 offset:16384\n"
    "ds_read_b128 v[64:67], v8 offset:18432\n"
    "s_waitcnt lgkmcnt(14)\n"
    "v_mfma_f32_16x16x32_bf16 v[16:19], v[84:87], a[120:123], v[16:19]\n"
    "v_mfma_f32_16x16x32_bf16 v[20:23], v[84:87], a[124:127], v[20:23]\n"
    "s_waitcnt lgkmcnt(12)\n"
    "v_mfma_f32_16x16x32_bf16 v[24:27], v[88:91], a[120:123], v[24:27]\n"
    "v_mfma_f32_16x16x32_bf16 v[28:31], v[88:91], a[124:127], v[28:31]\n"
    "s_waitcnt lgkmcnt(10)\n"
    "v_mfma_f32_16x16x32_bf16 v[32:35], v[92:95], a[120:123], v[32:35]\n"
    "v_mfma_f32_16x16x32_bf16 v[36:39], v[92:95], a[124:127], v[36:39]\n"
    "s_waitcnt lgkmcnt(6)\n"
    "v_mfma_f32_16x16x32_bf16 v[16:19], v[96:99], v[52:55], v[16:19]\n"
    "ds_read_b64_tr_b16 v[108:109], v9 offset:43520\n"
    "v_mfma_f32_16x16x32_bf16 v[20:23], v[96:99], v[56:59], v[20:23]\n"
    "ds_read_b64_tr_b16 v[110:111], v9 offset:43600\n"
    "s_waitcnt lgkmcnt(6)\n"
    "v_mfma_f32_16x16x32_bf16 v[24:27], v[100:103], v[52:55], v[24:27]\n"
    "ds_read_b64_tr_b16 v[112:113], v9 offset:43536\n"
    "v_mfma_f32_16x16x32_bf16 v[28:31], v[100:103], v[56:59], v[28:31]\n"
    "ds_read_b64_tr_b16 v[114:115], v9 offset:43616\n"
    "s_waitcnt lgkmcnt(6)\n"
    "v_mfma_f32_16x16x32_bf16 v[32:35], v[104:107], v[52:55], v[32:35]\n"
    "ds_read_b64_tr_b16 v[116:117], v10 offset:43520\n"
    "v_mfma_f32_16x16x32_bf16 v[36:39], v[104:107], v[56:59], v[36:39]\n"
    "ds_read_b64_tr_b16 v[118:119], v10 offset:43600\n"
    "s_lshr_b32 s15, s6, 3\n"
    "s_lshl_b32 s10, s5, 2\n"
    "s_add_u32 s10, s10, s15\n"
    "s_mul_i32 s15, s15, 3\n"
    "s_add_u32 s15, s15, s11\n"
    "s_add_u32 s15, s15, s5\n"
    "s_mul_i32 s16, s15, 26\n"
    "s_lshr_b32 s16, s16, 8\n"
    "s_mul_i32 s16, s16, 10\n"
    "s_sub_u32 s12, s15, s16\n"
    "s_barrier\n"
    "ds_read_b128 v[68:71], v7 offset:32768\n"
    "ds_read_b128 v[72:75], v7 offset:34816\n"
    "ds_read_b64_tr_b16 v[84:85], v9 offset:46080\n"
    "ds_read_b64_tr_b16 v[86:87], v9 offset:46160\n"
    "ds_read_b64_tr_b16 v[88:89], v9 offset:46096\n"
    "ds_read_b64_tr_b16 v[90:91], v9 offset:46176\n"
    "ds_read_b64_tr_b16 v[92:93], v10 offset:46080\n"
    "ds_read_b64_tr_b16 v[94:95], v10 offset:46160\n"
    "ds_read_b128 v[76:79], v8 offset:32768\n"
    "ds_read_b128 v[80:83], v8 offset:34816\n"
    "s_waitcnt lgkmcnt(14)\n"
    "v_mfma_f32_16x16x32_bf16 v[16:19], v[108:111], v[60:63], v[16:19]\n"
    "v_mfma_f32_16x16x32_bf16 v[20:23], v[108:111], v[64:67], v[20:23]\n"
    "s_waitcnt lgkmcnt(12)\n"
    "v_mfma_f32_16x16x32_bf16 v[24:27], v[112:115], v[60:63], v[24:27]\n"
    "v_mfma_f32_16x16x32_bf16 v[28:31], v[112:115], v[64:67], v[28:31]\n"
    "s_waitcnt lgkmcnt(10)\n"
    "v_mfma_f32_16x16x32_bf16 v[32:35], v[116:119], v[60:63], v[32:35]\n"
    "v_mfma_f32_16x16x32_bf16 v[36:39], v[116:119], v[64:67], v[36:39]\n"
    "s_waitcnt lgkmcnt(6)\n"
    "v_mfma_f32_16x16x32_bf16 v[16:19], v[84:87], v[68:71], v[16:19]\n"
    "ds_read_b64_tr_b16 v[96:97], v9 offset:48640\n"
    "v_mfma_f32_16x16x32_bf16 v[20:23], v[84:87], v[72:75], v[20:23]\n"
    "ds_read_b64_tr_b16 v[98:99], v9 offset:48720\n"
    "s_waitcnt lgkmcnt(6)\n"
    "v_mfma_f32_16x16x32_bf16 v[24:27], v[88:91], v[68:71], v[24:27]\n"
    "ds_read_b64_tr_b16 v[100:101], v9 offset:48656\n"
    "v_mfma_f32_16x16x32_bf16 v[28:31], v[88:91], v[72:75], v[28:31]\n"
    "ds_read_b64_tr_b16 v[102:103], v9 offset:48736\n"
    "s_waitcnt lgkmcnt(6)\n"
    "v_mfma_f32_16x16x32_bf16 v[32:35], v[92:95], v[68:71], v[32:35]\n"
    "ds_read_b64_tr_b16 v[104:105], v10 offset:48640\n"
    "v_mfma_f32_16x16x32_bf16 v[36:39], v[92:95], v[72:75], v[36:39]\n"
    "ds_read_b64_tr_b16 v[106:107], v10 offset:48720\n"
    "s_waitcnt lgkmcnt(4)\n"
    "v_mfma_f32_16x16x32_bf16 v[16:19], v[96:99], v[76:79], v[16:19]\n"
    "v_mfma_f32_16x16x32_bf16 v[20:23], v[96:99], v[80:83], v[20:23]\n"
    "s_waitcnt lgkmcnt(2)\n"
    "v_mfma_f32_16x16x32_bf16 v[24:27], v[100:103], v[76:79], v[24:27]\n"
    "v_mfma_f32_16x16x32_bf16 v[28:31], v[100:103], v[80:83], v[28:31]\n"
    "s_waitcnt lgkmcnt(0)\n"
    "v_mfma_f32_16x16x32_bf16 v[32:35], v[104:107], v[76:79], v[32:35]\n"
    "v_mfma_f32_16x16x32_bf16 v[36:39], v[104:107], v[80:83], v[36:39]\n"
    "s_setprio 1\n"
    "v_cvt_pk_bf16_f32 v40, v16, v17\n"
    "v_cvt_pk_bf16_f32 v42, v20, v21\n"
    "v_cvt_pk_bf16_f32 v41, v18, v19\n"
    "v_cvt_pk_bf16_f32 v43, v22, v23\n"
    "v_cvt_pk_bf16_f32 v44, v24, v25\n"
    "v_cvt_pk_bf16_f32 v46, v28, v29\n"
    "v_cvt_pk_bf16_f32 v45, v26, v27\n"
    "v_cvt_pk_bf16_f32 v47, v30, v31\n"
    "v_cvt_pk_bf16_f32 v48, v32, v33\n"
    "v_cvt_pk_bf16_f32 v50, v36, v37\n"
    "v_cvt_pk_bf16_f32 v49, v34, v35\n"
    "v_cvt_pk_bf16_f32 v51, v38, v39\n"
    "ds_read_b64_tr_b16 v[16:17], v2 offset:0\n"
    "v_permlane16_swap_b32 v40, v42\n"
    "ds_read_b64_tr_b16 v[28:29], v4 offset:0\n"
    "v_permlane16_swap_b32 v41, v43\n"
    "ds_read_b64_tr_b16 v[18:19], v2 offset:160\n"
    "v_permlane16_swap_b32 v44, v46\n"
    "ds_read_b64_tr_b16 v[30:31], v4 offset:160\n"
    "v_permlane16_swap_b32 v45, v47\n"
    "ds_read_b64_tr_b16 v[20:21], v2 offset:320\n"
    "v_permlane16_swap_b32 v48, v50\n"
    "ds_read_b64_tr_b16 v[32:33], v4 offset:320\n"
    "v_permlane16_swap_b32 v49, v51\n"
    "ds_read_b64_tr_b16 v[22:23], v2 offset:480\n"
    "ds_read_b64_tr_b16 v[34:35], v4 offset:480\n"
    "ds_read_b64_tr_b16 v[24:25], v3 offset:0\n"
    "ds_read_b64_tr_b16 v[36:37], v5 offset:0\n"
    "v_mov_b32 v26, 0\n"
    "v_mov_b32 v27, 0\n"
    "v_mov_b32 v38, 0\n"
    "v_mov_b32 v39, 0\n"
    "s_waitcnt lgkmcnt(7)\n"
    "v_mfma_f32_32x32x16_bf16 v[84:99], v[16:19], v[40:43], 0\n"
    "s_waitcnt lgkmcnt(6)\n"
    "v_mfma_f32_32x32x16_bf16 v[100:115], v[28:31], v[40:43], 0\n"
    "s_waitcnt lgkmcnt(3)\n"
    "v_mfma_f32_32x32x16_bf16 v[84:99], v[20:23], v[44:47], v[84:99]\n"
    "s_waitcnt lgkmcnt(2)\n"
    "v_mfma_f32_32x32x16_bf16 v[100:115], v[32:35], v[44:47], v[100:115]\n"
    "s_waitcnt lgkmcnt(1)\n"
    "v_mfma_f32_32x32x16_bf16 v[84:99], v[24:27], v[48:51], v[84:99]\n"
    "s_waitcnt lgkmcnt(0)\n"
    "v_mfma_f32_32x32x16_bf16 v[100:115], v[36:39], v[48:51], v[100:115]\n"
    "s_nop 11\n"
    "v_max3_f32 v104, v84, v85, v86\n"
    "v_min3_f32 v106, v84, v85, v86\n"
    "v_max3_f32 v104, v104, v87, v88\n"
    "v_min3_f32 v106, v106, v87, v88\n"
    "v_max3_f32 v104, v104, v89, v90\n"
    "v_min3_f32 v106, v106, v89, v90\n"
    "v_max3_f32 v104, v104, v91, v92\n"
    "v_min3_f32 v106, v106, v91, v92\n"
    "v_max3_f32 v104, v104, v93, v94\n"
    "v_min3_f32 v106, v106, v93, v94\n"
    "v_max3_f32 v104, v104, v95, v96\n"
    "v_min3_f32 v106, v106, v95, v96\n"
    "v_max3_f32 v104, v104, v97, v98\n"
    "v_min3_f32 v106, v106, v97, v98\n"
    "v_max3_f32 v104, v104, v99, v100\n"
    "v_min3_f32 v106, v106, v99, v100\n"
    "v_max3_f32 v104, v104, v101, v102\n"
    "v_min3_f32 v106, v106, v101, v102\n"
    "v_max_f32 v104, v104, v103\n"
    "v_min_f32 v106, v106, v103\n"
    "v_mul_f32_e64 v104, -v104, s78\n"
    "v_mul_f32 v106, s78, v106\n"
    "v_pk_fma_f32 v[16:17], v[84:85], s[78:79], v[104:105] op_sel_hi:[1,0,0]\n"
    "v_pk_fma_f32 v[84:85], v[84:85], s[78:79], v[106:107] op_sel_hi:[1,0,0] neg_lo:[0,1,0] neg_hi:[0,1,0]\n"
    "v_pk_fma_f32 v[18:19], v[86:87], s[78:79], v[104:105] op_sel_hi:[1,0,0]\n"
    "v_pk_fma_f32 v[86:87], v[86:87], s[78:79], v[106:107] op_sel_hi:[1,0,0] neg_lo:[0,1,0] neg_hi:[0,1,0]\n"
    "v_pk_fma_f32 v[20:21], v[88:89], s[78:79], v[104:105] op_sel_hi:[1,0,0]\n"
    "v_pk_fma_f32 v[88:89], v[88:89], s[78:79], v[106:107] op_sel_hi:[1,0,0] neg_lo:[0,1,0] neg_hi:[0,1,0]\n"
    "v_pk_fma_f32 v[22:23], v[90:91], s[78:79], v[104:105] op_sel_hi:[1,0,0]\n"
    "v_pk_fma_f32 v[90:91], v[90:91], s[78:79], v[106:107] op_sel_hi:[1,0,0] neg_lo:[0,1,0] neg_hi:[0,1,0]\n"
    "v_pk_fma_f32 v[24:25], v[92:93], s[78:79], v[104:105] op_sel_hi:[1,0,0]\n"
    "v_pk_fma_f32 v[92:93], v[92:93], s[78:79], v[106:107] op_sel_hi:[1,0,0] neg_lo:[0,1,0] neg_hi:[0,1,0]\n"
    "v_pk_fma_f32 v[26:27], v[94:95], s[78:79], v[104:105] op_sel_hi:[1,0,0]\n"
    "v_pk_fma_f32 v[94:95], v[94:95], s[78:79], v[106:107] op_sel_hi:[1,0,0] neg_lo:[0,1,0] neg_hi:[0,1,0]\n"
    "v_pk_fma_f32 v[28:29], v[96:97], s[78:79], v[104:105] op_sel_hi:[1,0,0]\n"
    "v_pk_fma_f32 v[96:97], v[96:97], s[78:79], v[106:107] op_sel_hi:[1,0,0] neg_lo:[0,1,0] neg_hi:[0,1,0]\n"
    "v_pk_fma_f32 v[30:31], v[98:99], s[78:79], v[104:105] op_sel_hi:[1,0,0]\n"
    "v_pk_fma_f32 v[98:99], v[98:99], s[78:79], v[106:107] op_sel_hi:[1,0,0] neg_lo:[0,1,0] neg_hi:[0,1,0]\n"
    "v_pk_fma_f32 v[32:33], v[100:101], s[78:79], v[104:105] op_sel_hi:[1,0,0]\n"
    "v_pk_fma_f32 v[100:101], v[100:101], s[78:79], v[106:107] op_sel_hi:[1,0,0] neg_lo:[0,1,0] neg_hi:[0,1,0]\n"
    "v_pk_fma_f32 v[34:35], v[102:103], s[78:79], v[104:105] op_sel_hi:[1,0,0]\n"
    "v_pk_fma_f32 v[102:103], v[102:103], s[78:79], v[106:107] op_sel_hi:[1,0,0] neg_lo:[0,1,0] neg_hi:[0,1,0]\n"
    "v_exp_f32 v16, v16\n"
    "v_exp_f32 v17, v17\n"
    "v_exp_f32 v84, v84\n"
    "v_exp_f32 v85, v85\n"
    "v_exp_f32 v18, v18\n"
    "v_exp_f32 v19, v19\n"
    "v_exp_f32 v86, v86\n"
    "v_exp_f32 v87, v87\n"
    "v_exp_f32 v20, v20\n"
    "v_exp_f32 v21, v21\n"
    "v_exp_f32 v88, v88\n"
    "v_exp_f32 v89, v89\n"
    "v_pk_add_f32 v[108:109], v[16:17], v[18:19]\n"
    "v_pk_add_f32 v[110:111], v[84:85], v[86:87]\n"
    "v_exp_f32 v22, v22\n"
    "v_exp_f32 v23, v23\n"
    "v_exp_f32 v90, v90\n"
    "v_exp_f32 v91, v91\n"
    "v_pk_add_f32 v[108:109], v[108:109], v[20:21]\n"
    "v_pk_add_f32 v[110:111], v[110:111], v[88:89]\n"
    "v_exp_f32 v24, v24\n"
    "v_exp_f32 v25, v25\n"
    "v_exp_f32 v92, v92\n"
    "v_exp_f32 v93, v93\n"
    "v_pk_add_f32 v[108:109], v[108:109], v[22:23]\n"
    "v_pk_add_f32 v[110:111], v[110:111], v[90:91]\n"
    "v_exp_f32 v26, v26\n"
    "v_exp_f32 v27, v27\n"
    "v_exp_f32 v94, v94\n"
    "v_exp_f32 v95, v95\n"
    "v_pk_add_f32 v[108:109], v[108:109], v[24:25]\n"
    "v_pk_add_f32 v[110:111], v[110:111], v[92:93]\n"
    "v_exp_f32 v28, v28\n"
    "v_exp_f32 v29, v29\n"
    "v_exp_f32 v96, v96\n"
    "v_exp_f32 v97, v97\n"
    "v_pk_add_f32 v[108:109], v[108:109], v[26:27]\n"
    "v_pk_add_f32 v[110:111], v[110:111], v[94:95]\n"
    "v_exp_f32 v30, v30\n"
    "v_exp_f32 v31, v31\n"
    "v_exp_f32 v98, v98\n"
    "v_exp_f32 v99, v99\n"
    "v_pk_add_f32 v[108:109], v[108:109], v[28:29]\n"
    "v_pk_add_f32 v[110:111], v[110:111], v[96:97]\n"
    "v_exp_f32 v32, v32\n"
    "v_exp_f32 v33, v33\n"
    "v_exp_f32 v100, v100\n"
    "v_exp_f32 v101, v101\n"
    "v_pk_add_f32 v[108:109], v[108:109], v[30:31]\n"
    "v_pk_add_f32 v[110:111], v[110:111], v[98:99]\n"
    "v_exp_f32 v34, v34\n"
    "v_exp_f32 v35, v35\n"
    "v_exp_f32 v102, v102\n"
    "v_exp_f32 v103, v103\n"
    "v_pk_add_f32 v[108:109], v[108:109], v[32:33]\n"
    "v_pk_add_f32 v[110:111], v[110:111], v[100:101]\n"
    "v_pk_add_f32 v[108:109], v[108:109], v[34:35]\n"
    "v_pk_add_f32 v[110:111], v[110:111], v[102:103]\n"
    "v_add_f32 v108, v108, v109\n"
    "v_add_f32 v110, v110, v111\n"
    "v_rcp_f32 v108, v108\n"
    "v_rcp_f32 v110, v110\n"
    "v_pk_mul_f32 v[16:17], v[16:17], v[108:109] op_sel_hi:[1,0]\n"
    "v_pk_mul_f32 v[18:19], v[18:19], v[108:109] op_sel_hi:[1,0]\n"
    "v_pk_mul_f32 v[20:21], v[20:21], v[108:109] op_sel_hi:[1,0]\n"
    "v_pk_mul_f32 v[22:23], v[22:23], v[108:109] op_sel_hi:[1,0]\n"
    "v_pk_mul_f32 v[24:25], v[24:25], v[108:109] op_sel_hi:[1,0]\n"
    "v_pk_mul_f32 v[26:27], v[26:27], v[108:109] op_sel_hi:[1,0]\n"
    "v_pk_mul_f32 v[28:29], v[28:29], v[108:109] op_sel_hi:[1,0]\n"
    "v_pk_mul_f32 v[30:31], v[30:31], v[108:109] op_sel_hi:[1,0]\n"
    "v_pk_mul_f32 v[32:33], v[32:33], v[108:109] op_sel_hi:[1,0]\n"
    "v_pk_mul_f32 v[34:35], v[34:35], v[108:109] op_sel_hi:[1,0]\n"
    "v_pk_mul_f32 v[84:85], v[84:85], v[110:111] op_sel_hi:[1,0]\n"
    "v_pk_mul_f32 v[86:87], v[86:87], v[110:111] op_sel_hi:[1,0]\n"
    "v_pk_mul_f32 v[88:89], v[88:89], v[110:111] op_sel_hi:[1,0]\n"
    "v_pk_mul_f32 v[90:91], v[90:91], v[110:111] op_sel_hi:[1,0]\n"
    "v_pk_mul_f32 v[92:93], v[92:93], v[110:111] op_sel_hi:[1,0]\n"
    "v_pk_mul_f32 v[94:95], v[94:95], v[110:111] op_sel_hi:[1,0]\n"
    "v_pk_mul_f32 v[96:97], v[96:97], v[110:111] op_sel_hi:[1,0]\n"
    "v_pk_mul_f32 v[98:99], v[98:99], v[110:111] op_sel_hi:[1,0]\n"
    "v_pk_mul_f32 v[100:101], v[100:101], v[110:111] op_sel_hi:[1,0]\n"
    "v_pk_mul_f32 v[102:103], v[102:103], v[110:111] op_sel_hi:[1,0]\n"
    "v_cvt_pk_bf16_f32 v16, v16, v17\n"
    "v_cvt_pk_bf16_f32 v17, v18, v19\n"
    "v_cvt_pk_bf16_f32 v18, v20, v21\n"
    "v_cvt_pk_bf16_f32 v19, v22, v23\n"
    "v_cvt_pk_bf16_f32 v20, v24, v25\n"
    "v_cvt_pk_bf16_f32 v21, v26, v27\n"
    "v_cvt_pk_bf16_f32 v22, v28, v29\n"
    "v_cvt_pk_bf16_f32 v23, v30, v31\n"
    "v_cvt_pk_bf16_f32 v24, v32, v33\n"
    "v_cvt_pk_bf16_f32 v25, v34, v35\n"
    "v_cvt_pk_bf16_f32 v26, v84, v85\n"
    "v_cvt_pk_bf16_f32 v27, v86, v87\n"
    "v_cvt_pk_bf16_f32 v28, v88, v89\n"
    "v_cvt_pk_bf16_f32 v29, v90, v91\n"
    "v_cvt_pk_bf16_f32 v30, v92, v93\n"
    "v_cvt_pk_bf16_f32 v31, v94, v95\n"
    "v_cvt_pk_bf16_f32 v32, v96, v97\n"
    "v_cvt_pk_bf16_f32 v33, v98, v99\n"
    "v_cvt_pk_bf16_f32 v34, v100, v101\n"
    "v_cvt_pk_bf16_f32 v35, v102, v103\n"
    "v_add_u32 v9, 51200, v9\n"
    "v_add_u32 v10, 51200, v10\n"
    "s_barrier\n"
    "ds_write_b128 v15, v[16:19] offset:0\n"
    "ds_write_b128 v15, v[20:23] offset:16\n"
    "ds_write_b128 v15, v[24:27] offset:32\n"
    "ds_write_b128 v15, v[28:31] offset:48\n"
    "ds_write_b128 v15, v[32:35] offset:64\n"
    "ds_read_b64_tr_b16 v[84:85], v9 offset:0\n"
    "ds_read_b64_tr_b16 v[86:87], v9 offset:80\n"
    "ds_read_b64_tr_b16 v[88:89], v9 offset:16\n"
    "ds_read_b64_tr_b16 v[90:91], v9 offset:96\n"
    "ds_read_b64_tr_b16 v[92:93], v10 offset:0\n"
    "ds_read_b64_tr_b16 v[94:95], v10 offset:80\n"
    "ds_read_b64_tr_b16 v[96:97], v9 offset:2560\n"
    "ds_read_b64_tr_b16 v[98:99], v9 offset:2640\n"
    "ds_read_b64_tr_b16 v[100:101], v9 offset:2576\n"
    "ds_read_b64_tr_b16 v[102:103], v9 offset:2656\n"
    "ds_read_b64_tr_b16 v[104:105], v10 offset:2560\n"
    "ds_read_b64_tr_b16 v[106:107], v10 offset:2640\n"
    "s_waitcnt lgkmcnt(6)\n"
    "v_mfma_f32_16x16x32_bf16 v[16:19], v[84:87], a[0:3], 0\n"
    "ds_read_b64_tr_b16 v[108:109], v9 offset:5120\n"
    "v_mfma_f32_16x16x32_bf16 v[20:23], v[84:87], a[4:7], 0\n"
    "ds_read_b64_tr_b16 v[110:111], v9 offset:5200\n"
    "v_mfma_f32_16x16x32_bf16 v[24:27], v[88:91], a[0:3], 0\n"
    "ds_read_b64_tr_b16 v[112:113], v9 offset:5136\n"
    "v_mfma_f32_16x16x32_bf16 v[28:31], v[88:91], a[4:7], 0\n"
    "ds_read_b64_tr_b16 v[114:115], v9 offset:5216\n"
    "v_mfma_f32_16x16x32_bf16 v[32:35], v[92:95], a[0:3], 0\n"
    "ds_read_b64_tr_b16 v[116:117], v10 offset:5120\n"
    "v_mfma_f32_16x16x32_bf16 v[36:39], v[92:95], a[4:7], 0\n"
    "ds_read_b64_tr_b16 v[118:119], v10 offset:5200\n"
    "s_waitcnt lgkmcnt(6)\n"
    "v_mfma_f32_16x16x32_bf16 v[16:19], v[96:99], a[8:11], v[16:19]\n"
    "ds_read_b64_tr_b16 v[84:85], v9 offset:7680\n"
    "v_mfma_f32_16x16x32_bf16 v[20:23], v[96:99], a[12:15], v[20:23]\n"
    "ds_read_b64_tr_b16 v[86:87], v9 offset:7760\n"
    "v_mfma_f32_16x16x32_bf16 v[24:27], v[100:103], a[8:11], v[24:27]\n"
    "ds_read_b64_tr_b16 v[88:89], v9 offset:7696\n"
    "v_mfma_f32_16x16x32_bf16 v[28:31], v[100:103], a[12:15], v[28:31]\n"
    "ds_read_b64_tr_b16 v[90:91], v9 offset:7776\n"
    "v_mfma_f32_16x16x32_bf16 v[32:35], v[104:107], a[8:11], v[32:35]\n"
    "ds_read_b64_tr_b16 v[92:93], v10 offset:7680\n"
    "v_mfma_f32_16x16x32_bf16 v[36:39], v[104:107], a[12:15], v[36:39]\n"
    "ds_read_b64_tr_b16 v[94:95], v10 offset:7760\n"
    "s_waitcnt lgkmcnt(6)\n"
    "v_mfma_f32_16x16x32_bf16 v[16:19], v[108:111], a[16:19], v[16:19]\n"
    "ds_read_b64_tr_b16 v[96:97], v9 offset:10240\n"
    "v_mfma_f32_16x16x32_bf16 v[20:23], v[108:111], a[20:23], v[20:23]\n"
    "ds_read_b64_tr_b16 v[98:99], v9 offset:10320\n"
    "v_mfma_f32_16x16x32_bf16 v[24:27], v[112:115], a[16:19], v[24:27]\n"
    "ds_read_b64_tr_b16 v[100:101], v9 offset:10256\n"
    "v_mfma_f32_16x16x32_bf16 v[28:31], v[112:115], a[20:23], v[28:31]\n"
    "ds_read_b64_tr_b16 v[102:103], v9 offset:10336\n"
    "v_mfma_f32_16x16x32_bf16 v[32:35], v[116:119], a[16:19], v[32:35]\n"
    "ds_read_b64_tr_b16 v[104:105], v10 offset:10240\n"
    "v_mfma_f32_16x16x32_bf16 v[36:39], v[116:119], a[20:23], v[36:39]\n"
    "ds_read_b64_tr_b16 v[106:107], v10 offset:10320\n"
    "s_waitcnt lgkmcnt(6)\n"
    "v_mfma_f32_16x16x32_bf16 v[16:19], v[84:87], a[24:27], v[16:19]\n"
    "ds_read_b64_tr_b16 v[108:109], v9 offset:12800\n"
    "v_mfma_f32_16x16x32_bf16 v[20:23], v[84:87], a[28:31], v[20:23]\n"
    "ds_read_b64_tr_b16 v[110:111], v9 offset:12880\n"
    "v_mfma_f32_16x16x32_bf16 v[24:27], v[88:91], a[24:27], v[24:27]\n"
    "ds_read_b64_tr_b16 v[112:113], v9 offset:12816\n"
    "v_mfma_f32_16x16x32_bf16 v[28:31], v[88:91], a[28:31], v[28:31]\n"
    "ds_read_b64_tr_b16 v[114:115], v9 offset:12896\n"
    "v_mfma_f32_16x16x32_bf16 v[32:35], v[92:95], a[24:27], v[32:35]\n"
    "ds_read_b64_tr_b16 v[116:117], v10 offset:12800\n"
    "v_mfma_f32_16x16x32_bf16 v[36:39], v[92:95], a[28:31], v[36:39]\n"
    "ds_read_b64_tr_b16 v[118:119], v10 offset:12880\n"
    "s_waitcnt lgkmcnt(6)\n"
    "v_mfma_f32_16x16x32_bf16 v[16:19], v[96:99], a[32:35], v[16:19]\n"
    "ds_read_b64_tr_b16 v[84:85], v9 offset:15360\n"
    "v_mfma_f32_16x16x32_bf16 v[20:23], v[96:99], a[36:39], v[20:23]\n"
    "ds_read_b64_tr_b16 v[86:87], v9 offset:15440\n"
    "v_mfma_f32_16x16x32_bf16 v[24:27], v[100:103], a[32:35], v[24:27]\n"
    "ds_read_b64_tr_b16 v[88:89], v9 offset:15376\n"
    "v_mfma_f32_16x16x32_bf16 v[28:31], v[100:103], a[36:39], v[28:31]\n"
    "ds_read_b64_tr_b16 v[90:91], v9 offset:15456\n"
    "v_mfma_f32_16x16x32_bf16 v[32:35], v[104:107], a[32:35], v[32:35]\n"
    "ds_read_b64_tr_b16 v[92:93], v10 offset:15360\n"
    "v_mfma_f32_16x16x32_bf16 v[36:39], v[104:107], a[36:39], v[36:39]\n"
    "ds_read_b64_tr_b16 v[94:95], v10 offset:15440\n"
    "s_waitcnt lgkmcnt(6)\n"
    "v_mfma_f32_16x16x32_bf16 v[16:19], v[108:111], a[40:43], v[16:19]\n"
    "ds_read_b64_tr_b16 v[96:97], v9 offset:17920\n"
    "v_mfma_f32_16x16x32_bf16 v[20:23], v[108:111], a[44:47], v[20:23]\n"
    "ds_read_b64_tr_b16 v[98:99], v9 offset:18000\n"
    "v_mfma_f32_16x16x32_bf16 v[24:27], v[112:115], a[40:43], v[24:27]\n"
    "ds_read_b64_tr_b16 v[100:101], v9 offset:17936\n"
    "v_mfma_f32_16x16x32_bf16 v[28:31], v[112:115], a[44:47], v[28:31]\n"
    "ds_read_b64_tr_b16 v[102:103], v9 offset:18016\n"
    "v_mfma_f32_16x16x32_bf16 v[32:35], v[116:119], a[40:43], v[32:35]\n"
    "ds_read_b64_tr_b16 v[104:105], v10 offset:17920\n"
    "v_mfma_f32_16x16x32_bf16 v[36:39], v[116:119], a[44:47], v[36:39]\n"
    "ds_read_b64_tr_b16 v[106:107], v10 offset:18000\n"
    "s_waitcnt lgkmcnt(6)\n"
    "v_mfma_f32_16x16x32_bf16 v[16:19], v[84:87], a[48:51], v[16:19]\n"
    "ds_read_b64_tr_b16 v[108:109], v9 offset:20480\n"
    "v_mfma_f32_16x16x32_bf16 v[20:23], v[84:87], a[52:55], v[20:23]\n"
    "ds_read_b64_tr_b16 v[110:111], v9 offset:20560\n"
    "v_mfma_f32_16x16x32_bf16 v[24:27], v[88:91], a[48:51], v[24:27]\n"
    "ds_read_b64_tr_b16 v[112:113], v9 offset:20496\n"
    "v_mfma_f32_16x16x32_bf16 v[28:31], v[88:91], a[52:55], v[28:31]\n"
    "ds_read_b64_tr_b16 v[114:115], v9 offset:20576\n"
    "v_mfma_f32_16x16x32_bf16 v[32:35], v[92:95], a[48:51], v[32:35]\n"
    "ds_read_b64_tr_b16 v[116:117], v10 offset:20480\n"
    "v_mfma_f32_16x16x32_bf16 v[36:39], v[92:95], a[52:55], v[36:39]\n"
    "ds_read_b64_tr_b16 v[118:119], v10 offset:20560\n"
    "s_waitcnt lgkmcnt(6)\n"
    "v_mfma_f32_16x16x32_bf16 v[16:19], v[96:99], a[56:59], v[16:19]\n"
    "ds_read_b64_tr_b16 v[84:85], v9 offset:23040\n"
    "v_mfma_f32_16x16x32_bf16 v[20:23], v[96:99], a[60:63], v[20:23]\n"
    "ds_read_b64_tr_b16 v[86:87], v9 offset:23120\n"
    "v_mfma_f32_16x16x32_bf16 v[24:27], v[100:103], a[56:59], v[24:27]\n"
    "ds_read_b64_tr_b16 v[88:89], v9 offset:23056\n"
    "v_mfma_f32_16x16x32_bf16 v[28:31], v[100:103], a[60:63], v[28:31]\n"
    "ds_read_b64_tr_b16 v[90:91], v9 offset:23136\n"
    "v_mfma_f32_16x16x32_bf16 v[32:35], v[104:107], a[56:59], v[32:35]\n"
    "ds_read_b64_tr_b16 v[92:93], v10 offset:23040\n"
    "v_mfma_f32_16x16x32_bf16 v[36:39], v[104:107], a[60:63], v[36:39]\n"
    "ds_read_b64_tr_b16 v[94:95], v10 offset:23120\n"
    "s_waitcnt lgkmcnt(6)\n"
    "s_barrier\n"
    "v_mfma_f32_16x16x32_bf16 v[16:19], v[108:111], a[64:67], v[16:19]\n"
    "ds_read_b64_tr_b16 v[96:97], v9 offset:25600\n"
    "v_mfma_f32_16x16x32_bf16 v[20:23], v[108:111], a[68:71], v[20:23]\n"
    "ds_read_b64_tr_b16 v[98:99], v9 offset:25680\n"
    "v_mfma_f32_16x16x32_bf16 v[24:27], v[112:115], a[64:67], v[24:27]\n"
    "ds_read_b64_tr_b16 v[100:101], v9 offset:25616\n"
    "v_mfma_f32_16x16x32_bf16 v[28:31], v[112:115], a[68:71], v[28:31]\n"
    "ds_read_b64_tr_b16 v[102:103], v9 offset:25696\n"
    "v_mfma_f32_16x16x32_bf16 v[32:35], v[116:119], a[64:67], v[32:35]\n"
    "ds_read_b64_tr_b16 v[104:105], v10 offset:25600\n"
    "v_mfma_f32_16x16x32_bf16 v[36:39], v[116:119], a[68:71], v[36:39]\n"
    "ds_read_b64_tr_b16 v[106:107], v10 offset:25680\n"
    "s_waitcnt lgkmcnt(6)\n"
    "v_mfma_f32_16x16x32_bf16 v[16:19], v[84:87], a[72:75], v[16:19]\n"
    "ds_read_b64_tr_b16 v[108:109], v9 offset:28160\n"
    "v_mfma_f32_16x16x32_bf16 v[20:23], v[84:87], a[76:79], v[20:23]\n"
    "ds_read_b64_tr_b16 v[110:111], v9 offset:28240\n"
    "v_mfma_f32_16x16x32_bf16 v[24:27], v[88:91], a[72:75], v[24:27]\n"
    "ds_read_b64_tr_b16 v[112:113], v9 offset:28176\n"
    "v_mfma_f32_16x16x32_bf16 v[28:31], v[88:91], a[76:79], v[28:31]\n"
    "ds_read_b64_tr_b16 v[114:115], v9 offset:28256\n"
    "v_mfma_f32_16x16x32_bf16 v[32:35], v[92:95], a[72:75], v[32:35]\n"
    "ds_read_b64_tr_b16 v[116:117], v10 offset:28160\n"
    "v_mfma_f32_16x16x32_bf16 v[36:39], v[92:95], a[76:79], v[36:39]\n"
    "ds_read_b64_tr_b16 v[118:119], v10 offset:28240\n"
    "s_waitcnt lgkmcnt(6)\n"
    "v_mfma_f32_16x16x32_bf16 v[16:19], v[96:99], a[80:83], v[16:19]\n"
    "ds_read_b64_tr_b16 v[84:85], v9 offset:30720\n"
    "v_mfma_f32_16x16x32_bf16 v[20:23], v[96:99], a[84:87], v[20:23]\n"
    "ds_read_b64_tr_b16 v[86:87], v9 offset:30800\n"
    "v_mfma_f32_16x16x32_bf16 v[24:27], v[100:103], a[80:83], v[24:27]\n"
    "ds_read_b64_tr_b16 v[88:89], v9 offset:30736\n"
    "v_mfma_f32_16x16x32_bf16 v[28:31], v[100:103], a[84:87], v[28:31]\n"
    "ds_read_b64_tr_b16 v[90:91], v9 offset:30816\n"
    "v_mfma_f32_16x16x32_bf16 v[32:35], v[104:107], a[80:83], v[32:35]\n"
    "ds_read_b64_tr_b16 v[92:93], v10 offset:30720\n"
    "v_mfma_f32_16x16x32_bf16 v[36:39], v[104:107], a[84:87], v[36:39]\n"
    "ds_read_b64_tr_b16 v[94:95], v10 offset:30800\n"
    "s_waitcnt lgkmcnt(6)\n"
    "v_mfma_f32_16x16x32_bf16 v[16:19], v[108:111], a[88:91], v[16:19]\n"
    "ds_read_b64_tr_b16 v[96:97], v9 offset:33280\n"
    "v_mfma_f32_16x16x32_bf16 v[20:23], v[108:111], a[92:95], v[20:23]\n"
    "ds_read_b64_tr_b16 v[98:99], v9 offset:33360\n"
    "v_mfma_f32_16x16x32_bf16 v[24:27], v[112:115], a[88:91], v[24:27]\n"
    "ds_read_b64_tr_b16 v[100:101], v9 offset:33296\n"
    "v_mfma_f32_16x16x32_bf16 v[28:31], v[112:115], a[92:95], v[28:31]\n"
    "ds_read_b64_tr_b16 v[102:103], v9 offset:33376\n"
    "v_mfma_f32_16x16x32_bf16 v[32:35], v[116:119], a[88:91], v[32:35]\n"
    "ds_read_b64_tr_b16 v[104:105], v10 offset:33280\n"
    "v_mfma_f32_16x16x32_bf16 v[36:39], v[116:119], a[92:95], v[36:39]\n"
    "ds_read_b64_tr_b16 v[106:107], v10 offset:33360\n"
    "s_waitcnt lgkmcnt(6)\n"
    "v_mfma_f32_16x16x32_bf16 v[16:19], v[84:87], a[96:99], v[16:19]\n"
    "ds_read_b64_tr_b16 v[108:109], v9 offset:35840\n"
    "v_mfma_f32_16x16x32_bf16 v[20:23], v[84:87], a[100:103], v[20:23]\n"
    "ds_read_b64_tr_b16 v[110:111], v9 offset:35920\n"
    "v_mfma_f32_16x16x32_bf16 v[24:27], v[88:91], a[96:99], v[24:27]\n"
    "ds_read_b64_tr_b16 v[112:113], v9 offset:35856\n"
    "v_mfma_f32_16x16x32_bf16 v[28:31], v[88:91], a[100:103], v[28:31]\n"
    "ds_read_b64_tr_b16 v[114:115], v9 offset:35936\n"
    "v_mfma_f32_16x16x32_bf16 v[32:35], v[92:95], a[96:99], v[32:35]\n"
    "ds_read_b64_tr_b16 v[116:117], v10 offset:35840\n"
    "v_mfma_f32_16x16x32_bf16 v[36:39], v[92:95], a[100:103], v[36:39]\n"
    "ds_read_b64_tr_b16 v[118:119], v10 offset:35920\n"
    "s_waitcnt lgkmcnt(6)\n"
    "v_mfma_f32_16x16x32_bf16 v[16:19], v[96:99], a[104:107], v[16:19]\n"
    "ds_read_b64_tr_b16 v[84:85], v9 offset:38400\n"
    "v_mfma_f32_16x16x32_bf16 v[20:23], v[96:99], a[108:111], v[20:23]\n"
    "ds_read_b64_tr_b16 v[86:87], v9 offset:38480\n"
    "v_mfma_f32_16x16x32_bf16 v[24:27], v[100:103], a[104:107], v[24:27]\n"
    "ds_read_b64_tr_b16 v[88:89], v9 offset:38416\n"
    "v_mfma_f32_16x16x32_bf16 v[28:31], v[100:103], a[108:111], v[28:31]\n"
    "ds_read_b64_tr_b16 v[90:91], v9 offset:38496\n"
    "v_mfma_f32_16x16x32_bf16 v[32:35], v[104:107], a[104:107], v[32:35]\n"
    "ds_read_b64_tr_b16 v[92:93], v10 offset:38400\n"
    "v_mfma_f32_16x16x32_bf16 v[36:39], v[104:107], a[108:111], v[36:39]\n"
    "ds_read_b64_tr_b16 v[94:95], v10 offset:38480\n"
    "s_waitcnt lgkmcnt(6)\n"
    "v_mfma_f32_16x16x32_bf16 v[16:19], v[108:111], a[112:115], v[16:19]\n"
    "ds_read_b64_tr_b16 v[96:97], v9 offset:40960\n"
    "v_mfma_f32_16x16x32_bf16 v[20:23], v[108:111], a[116:119], v[20:23]\n"
    "ds_read_b64_tr_b16 v[98:99], v9 offset:41040\n"
    "v_mfma_f32_16x16x32_bf16 v[24:27], v[112:115], a[112:115], v[24:27]\n"
    "ds_read_b64_tr_b16 v[100:101], v9 offset:40976\n"
    "v_mfma_f32_16x16x32_bf16 v[28:31], v[112:115], a[116:119], v[28:31]\n"
    "ds_read_b64_tr_b16 v[102:103], v9 offset:41056\n"
    "v_mfma_f32_16x16x32_bf16 v[32:35], v[116:119], a[112:115], v[32:35]\n"
    "ds_read_b64_tr_b16 v[104:105], v10 offset:40960\n"
    "v_mfma_f32_16x16x32_bf16 v[36:39], v[116:119], a[116:119], v[36:39]\n"
    "ds_read_b64_tr_b16 v[106:107], v10 offset:41040\n"
    "s_waitcnt lgkmcnt(6)\n"
    "v_mfma_f32_16x16x32_bf16 v[16:19], v[84:87], a[120:123], v[16:19]\n"
    "ds_read_b64_tr_b16 v[108:109], v9 offset:43520\n"
    "v_mfma_f32_16x16x32_bf16 v[20:23], v[84:87], a[124:127], v[20:23]\n"
    "ds_read_b64_tr_b16 v[110:111], v9 offset:43600\n"
    "v_mfma_f32_16x16x32_bf16 v[24:27], v[88:91], a[120:123], v[24:27]\n"
    "ds_read_b64_tr_b16 v[112:113], v9 offset:43536\n"
    "v_mfma_f32_16x16x32_bf16 v[28:31], v[88:91], a[124:127], v[28:31]\n"
    "ds_read_b64_tr_b16 v[114:115], v9 offset:43616\n"
    "v_mfma_f32_16x16x32_bf16 v[32:35], v[92:95], a[120:123], v[32:35]\n"
    "ds_read_b64_tr_b16 v[116:117], v10 offset:43520\n"
    "v_mfma_f32_16x16x32_bf16 v[36:39], v[92:95], a[124:127], v[36:39]\n"
    "ds_read_b64_tr_b16 v[118:119], v10 offset:43600\n"
    "s_waitcnt lgkmcnt(6)\n"
    "s_barrier\n"
    "v_mfma_f32_16x16x32_bf16 v[16:19], v[96:99], v[52:55], v[16:19]\n"
    "ds_read_b64_tr_b16 v[84:85], v9 offset:46080\n"
    "v_mfma_f32_16x16x32_bf16 v[20:23], v[96:99], v[56:59], v[20:23]\n"
    "ds_read_b64_tr_b16 v[86:87], v9 offset:46160\n"
    "v_mfma_f32_16x16x32_bf16 v[24:27], v[100:103], v[52:55], v[24:27]\n"
    "ds_read_b64_tr_b16 v[88:89], v9 offset:46096\n"
    "v_mfma_f32_16x16x32_bf16 v[28:31], v[100:103], v[56:59], v[28:31]\n"
    "ds_read_b64_tr_b16 v[90:91], v9 offset:46176\n"
    "v_mfma_f32_16x16x32_bf16 v[32:35], v[104:107], v[52:55], v[32:35]\n"
    "ds_read_b64_tr_b16 v[92:93], v10 offset:46080\n"
    "v_mfma_f32_16x16x32_bf16 v[36:39], v[104:107], v[56:59], v[36:39]\n"
    "ds_read_b64_tr_b16 v[94:95], v10 offset:46160\n"
    "s_waitcnt lgkmcnt(6)\n"
    "v_mfma_f32_16x16x32_bf16 v[16:19], v[108:111], v[60:63], v[16:19]\n"
    "ds_read_b64_tr_b16 v[96:97], v9 offset:48640\n"
    "v_mfma_f32_16x16x32_bf16 v[20:23], v[108:111], v[64:67], v[20:23]\n"
    "ds_read_b64_tr_b16 v[98:99], v9 offset:48720\n"
    "v_mfma_f32_16x16x32_bf16 v[24:27], v[112:115], v[60:63], v[24:27]\n"
    "ds_read_b64_tr_b16 v[100:101], v9 offset:48656\n"
    "v_mfma_f32_16x16x32_bf16 v[28:31], v[112:115], v[64:67], v[28:31]\n"
    "ds_read_b64_tr_b16 v[102:103], v9 offset:48736\n"
    "v_mfma_f32_16x16x32_bf16 v[32:35], v[116:119], v[60:63], v[32:35]\n"
    "ds_read_b64_tr_b16 v[104:105], v10 offset:48640\n"
    "v_mfma_f32_16x16x32_bf16 v[36:39], v[116:119], v[64:67], v[36:39]\n"
    "ds_read_b64_tr_b16 v[106:107], v10 offset:48720\n"
    "s_waitcnt lgkmcnt(6)\n"
    "v_mfma_f32_16x16x32_bf16 v[16:19], v[84:87], v[68:71], v[16:19]\n"
    "v_mfma_f32_16x16x32_bf16 v[20:23], v[84:87], v[72:75], v[20:23]\n"
    "v_mfma_f32_16x16x32_bf16 v[24:27], v[88:91], v[68:71], v[24:27]\n"
    "v_mfma_f32_16x16x32_bf16 v[28:31], v[88:91], v[72:75], v[28:31]\n"
    "v_mfma_f32_16x16x32_bf16 v[32:35], v[92:95], v[68:71], v[32:35]\n"
    "v_mfma_f32_16x16x32_bf16 v[36:39], v[92:95], v[72:75], v[36:39]\n"
    "s_waitcnt lgkmcnt(0)\n"
    "v_mfma_f32_16x16x32_bf16 v[16:19], v[96:99], v[76:79], v[16:19]\n"
    "v_mfma_f32_16x16x32_bf16 v[20:23], v[96:99], v[80:83], v[20:23]\n"
    "v_mfma_f32_16x16x32_bf16 v[24:27], v[100:103], v[76:79], v[24:27]\n"
    "v_mfma_f32_16x16x32_bf16 v[28:31], v[100:103], v[80:83], v[28:31]\n"
    "v_mfma_f32_16x16x32_bf16 v[32:35], v[104:107], v[76:79], v[32:35]\n"
    "v_mfma_f32_16x16x32_bf16 v[36:39], v[104:107], v[80:83], v[36:39]\n"
    "s_nop 2\n"
    "v_cvt_pk_bf16_f32 v40, v16, v17\n"
    "v_cvt_pk_bf16_f32 v42, v20, v21\n"
    "v_cvt_pk_bf16_f32 v41, v18, v19\n"
    "v_cvt_pk_bf16_f32 v43, v22, v23\n"
    "v_cvt_pk_bf16_f32 v44, v24, v25\n"
    "v_cvt_pk_bf16_f32 v46, v28, v29\n"
    "v_cvt_pk_bf16_f32 v45, v26, v27\n"
    "v_permlane16_swap_b32 v40, v42\n"
    "v_cvt_pk_bf16_f32 v47, v30, v31\n"
    "v_cvt_pk_bf16_f32 v48, v32, v33\n"
    "v_permlane16_swap_b32 v41, v43\n"
    "v_cvt_pk_bf16_f32 v50, v36, v37\n"
    "v_cvt_pk_bf16_f32 v49, v34, v35\n"
    "v_permlane16_swap_b32 v44, v46\n"
    "v_cvt_pk_bf16_f32 v51, v38, v39\n"
    "v_permlane16_swap_b32 v45, v47\n"
    "v_permlane16_swap_b32 v48, v50\n"
    "v_permlane16_swap_b32 v49, v51\n"
    "v_mov_b32 v50, 0x3f80\n"
    "v_mov_b32 v51, 0\n"
    "ds_write_b128 v120, v[40:43] offset:0\n"
    "ds_write_b128 v120, v[44:47] offset:16\n"
    "ds_write_b128 v120, v[48:51] offset:32\n"
    "v_add_u32 v9, 51200, v9\n"
    "v_add_u32 v10, 51200, v10\n"
    "s_waitcnt lgkmcnt(0)\n"
    "s_barrier\n"
    "ds_read_b64_tr_b16 v[84:85], v9 offset:0\n"
    "ds_read_b64_tr_b16 v[86:87], v9 offset:80\n"
    "ds_read_b64_tr_b16 v[88:89], v9 offset:16\n"
    "ds_read_b64_tr_b16 v[90:91], v9 offset:96\n"
    "ds_read_b64_tr_b16 v[92:93], v10 offset:0\n"
    "ds_read_b64_tr_b16 v[94:95], v10 offset:80\n"
    "ds_read_b64_tr_b16 v[96:97], v9 offset:2560\n"
    "ds_read_b64_tr_b16 v[98:99], v9 offset:2640\n"
    "ds_read_b64_tr_b16 v[100:101], v9 offset:2576\n"
    "ds_read_b64_tr_b16 v[102:103], v9 offset:2656\n"
    "ds_read_b64_tr_b16 v[104:105], v10 offset:2560\n"
    "ds_read_b64_tr_b16 v[106:107], v10 offset:2640\n"
    "s_waitcnt lgkmcnt(6)\n"
    "v_mfma_f32_16x16x32_bf16 v[16:19], v[84:87], a[0:3], 0\n"
    "ds_read_b64_tr_b16 v[108:109], v9 offset:5120\n"
    "v_mfma_f32_16x16x32_bf16 v[20:23], v[84:87], a[4:7], 0\n"
    "ds_read_b64_tr_b16 v[110:111], v9 offset:5200\n"
    "v_mfma_f32_16x16x32_bf16 v[24:27], v[88:91], a[0:3], 0\n"
    "ds_read_b64_tr_b16 v[112:113], v9 offset:5136\n"
    "v_mfma_f32_16x16x32_bf16 v[28:31], v[88:91], a[4:7], 0\n"
    "ds_read_b64_tr_b16 v[114:115], v9 offset:5216\n"
    "v_mfma_f32_16x16x32_bf16 v[32:35], v[92:95], a[0:3], 0\n"
    "ds_read_b64_tr_b16 v[116:117], v10 offset:5120\n"
    "v_mfma_f32_16x16x32_bf16 v[36:39], v[92:95], a[4:7], 0\n"
    "ds_read_b64_tr_b16 v[118:119], v10 offset:5200\n"
    "s_waitcnt lgkmcnt(6)\n"
    "v_mfma_f32_16x16x32_bf16 v[16:19], v[96:99], a[8:11], v[16:19]\n"
    "ds_read_b64_tr_b16 v[84:85], v9 offset:7680\n"
    "v_mfma_f32_16x16x32_bf16 v[20:23], v[96:99], a[12:15], v[20:23]\n"
    "ds_read_b64_tr_b16 v[86:87], v9 offset:7760\n"
    "v_mfma_f32_16x16x32_bf16 v[24:27], v[100:103], a[8:11], v[24:27]\n"
    "ds_read_b64_tr_b16 v[88:89], v9 offset:7696\n"
    "v_mfma_f32_16x16x32_bf16 v[28:31], v[100:103], a[12:15], v[28:31]\n"
    "ds_read_b64_tr_b16 v[90:91], v9 offset:7776\n"
    "v_mfma_f32_16x16x32_bf16 v[32:35], v[104:107], a[8:11], v[32:35]\n"
    "ds_read_b64_tr_b16 v[92:93], v10 offset:7680\n"
    "v_mfma_f32_16x16x32_bf16 v[36:39], v[104:107], a[12:15], v[36:39]\n"
    "ds_read_b64_tr_b16 v[94:95], v10 offset:7760\n"
    "s_waitcnt lgkmcnt(6)\n"
    "v_mfma_f32_16x16x32_bf16 v[16:19], v[108:111], a[16:19], v[16:19]\n"
    "ds_read_b64_tr_b16 v[96:97], v9 offset:10240\n"
    "v_mfma_f32_16x16x32_bf16 v[20:23], v[108:111], a[20:23], v[20:23]\n"
    "ds_read_b64_tr_b16 v[98:99], v9 offset:10320\n"
    "v_mfma_f32_16x16x32_bf16 v[24:27], v[112:115], a[16:19], v[24:27]\n"
    "ds_read_b64_tr_b16 v[100:101], v9 offset:10256\n"
    "v_mfma_f32_16x16x32_bf16 v[28:31], v[112:115], a[20:23], v[28:31]\n"
    "ds_read_b64_tr_b16 v[102:103], v9 offset:10336\n"
    "v_mfma_f32_16x16x32_bf16 v[32:35], v[116:119], a[16:19], v[32:35]\n"
    "ds_read_b64_tr_b16 v[104:105], v10 offset:10240\n"
    "v_mfma_f32_16x16x32_bf16 v[36:39], v[116:119], a[20:23], v[36:39]\n"
    "ds_read_b64_tr_b16 v[106:107], v10 offset:10320\n"
    "s_waitcnt lgkmcnt(6)\n"
    "v_mfma_f32_16x16x32_bf16 v[16:19], v[84:87], a[24:27], v[16:19]\n"
    "ds_read_b64_tr_b16 v[108:109], v9 offset:12800\n"
    "v_mfma_f32_16x16x32_bf16 v[20:23], v[84:87], a[28:31], v[20:23]\n"
    "ds_read_b64_tr_b16 v[110:111], v9 offset:12880\n"
    "v_mfma_f32_16x16x32_bf16 v[24:27], v[88:91], a[24:27], v[24:27]\n"
    "ds_read_b64_tr_b16 v[112:113], v9 offset:12816\n"
    "v_mfma_f32_16x16x32_bf16 v[28:31], v[88:91], a[28:31], v[28:31]\n"
    "ds_read_b64_tr_b16 v[114:115], v9 offset:12896\n"
    "v_mfma_f32_16x16x32_bf16 v[32:35], v[92:95], a[24:27], v[32:35]\n"
    "ds_read_b64_tr_b16 v[116:117], v10 offset:12800\n"
    "v_mfma_f32_16x16x32_bf16 v[36:39], v[92:95], a[28:31], v[36:39]\n"
    "ds_read_b64_tr_b16 v[118:119], v10 offset:12880\n"
    "s_waitcnt lgkmcnt(6)\n"
    "v_mfma_f32_16x16x32_bf16 v[16:19], v[96:99], a[32:35], v[16:19]\n"
    "ds_read_b64_tr_b16 v[84:85], v9 offset:15360\n"
    "v_accvgpr_write_b32 a10, 0\n"
    "v_mfma_f32_16x16x32_bf16 v[20:23], v[96:99], a[36:39], v[20:23]\n"
    "ds_read_b64_tr_b16 v[86:87], v9 offset:15440\n"
    "v_accvgpr_write_b32 a11, 0\n"
    "v_mfma_f32_16x16x32_bf16 v[24:27], v[100:103], a[32:35], v[24:27]\n"
    "ds_read_b64_tr_b16 v[88:89], v9 offset:15376\n"
    "v_accvgpr_write_b32 a22, 0\n"
    "v_mfma_f32_16x16x32_bf16 v[28:31], v[100:103], a[36:39], v[28:31]\n"
    "ds_read_b64_tr_b16 v[90:91], v9 offset:15456\n"
    "v_accvgpr_write_b32 a23, 0\n"
    "v_mfma_f32_16x16x32_bf16 v[32:35], v[104:107], a[32:35], v[32:35]\n"
    "ds_read_b64_tr_b16 v[92:93], v10 offset:15360\n"
    "v_mfma_f32_16x16x32_bf16 v[36:39], v[104:107], a[36:39], v[36:39]\n"
    "ds_read_b64_tr_b16 v[94:95], v10 offset:15440\n"
    "s_waitcnt lgkmcnt(6)\n"
    "v_mfma_f32_16x16x32_bf16 v[16:19], v[108:111], a[40:43], v[16:19]\n"
    "ds_read_b64_tr_b16 v[96:97], v9 offset:17920\n"
    "v_mfma_f32_16x16x32_bf16 v[20:23], v[108:111], a[44:47], v[20:23]\n"
    "ds_read_b64_tr_b16 v[98:99], v9 offset:18000\n"
    "v_mfma_f32_16x16x32_bf16 v[24:27], v[112:115], a[40:43], v[24:27]\n"
    "ds_read_b64_tr_b16 v[100:101], v9 offset:17936\n"
    "v_mfma_f32_16x16x32_bf16 v[28:31], v[112:115], a[44:47], v[28:31]\n"
    "ds_read_b64_tr_b16 v[102:103], v9 offset:18016\n"
    "v_mfma_f32_16x16x32_bf16 v[32:35], v[116:119], a[40:43], v[32:35]\n"
    "ds_read_b64_tr_b16 v[104:105], v10 offset:17920\n"
    "v_mfma_f32_16x16x32_bf16 v[36:39], v[116:119], a[44:47], v[36:39]\n"
    "ds_read_b64_tr_b16 v[106:107], v10 offset:18000\n"
    "s_waitcnt lgkmcnt(6)\n"
    "s_barrier\n"
    "v_mfma_f32_16x16x32_bf16 v[16:19], v[84:87], a[48:51], v[16:19]\n"
    "ds_read_b64_tr_b16 v[108:109], v9 offset:20480\n"
    "v_mfma_f32_16x16x32_bf16 v[20:23], v[84:87], a[52:55], v[20:23]\n"
    "ds_read_b64_tr_b16 v[110:111], v9 offset:20560\n"
    "v_mfma_f32_16x16x32_bf16 v[24:27], v[88:91], a[48:51], v[24:27]\n"
    "ds_read_b64_tr_b16 v[112:113], v9 offset:20496\n"
    "v_mfma_f32_16x16x32_bf16 v[28:31], v[88:91], a[52:55], v[28:31]\n"
    "ds_read_b64_tr_b16 v[114:115], v9 offset:20576\n"
    "v_mfma_f32_16x16x32_bf16 v[32:35], v[92:95], a[48:51], v[32:35]\n"
    "ds_read_b64_tr_b16 v[116:117], v10 offset:20480\n"
    "v_mfma_f32_16x16x32_bf16 v[36:39], v[92:95], a[52:55], v[36:39]\n"
    "ds_read_b64_tr_b16 v[118:119], v10 offset:20560\n"
    "s_waitcnt lgkmcnt(6)\n"
    "v_mfma_f32_16x16x32_bf16 v[16:19], v[96:99], a[56:59], v[16:19]\n"
    "ds_read_b64_tr_b16 v[84:85], v9 offset:23040\n"
    "v_mfma_f32_16x16x32_bf16 v[20:23], v[96:99], a[60:63], v[20:23]\n"
    "ds_read_b64_tr_b16 v[86:87], v9 offset:23120\n"
    "v_mfma_f32_16x16x32_bf16 v[24:27], v[100:103], a[56:59], v[24:27]\n"
    "ds_read_b64_tr_b16 v[88:89], v9 offset:23056\n"
    "v_mfma_f32_16x16x32_bf16 v[28:31], v[100:103], a[60:63], v[28:31]\n"
    "ds_read_b64_tr_b16 v[90:91], v9 offset:23136\n"
    "v_mfma_f32_16x16x32_bf16 v[32:35], v[104:107], a[56:59], v[32:35]\n"
    "ds_read_b64_tr_b16 v[92:93], v10 offset:23040\n"
    "v_mfma_f32_16x16x32_bf16 v[36:39], v[104:107], a[60:63], v[36:39]\n"
    "ds_read_b64_tr_b16 v[94:95], v10 offset:23120\n"
    "s_waitcnt lgkmcnt(6)\n"
    "v_mfma_f32_16x16x32_bf16 v[16:19], v[108:111], a[64:67], v[16:19]\n"
    "ds_read_b64_tr_b16 v[96:97], v9 offset:25600\n"
    "v_mfma_f32_16x16x32_bf16 v[20:23], v[108:111], a[68:71], v[20:23]\n"
    "ds_read_b64_tr_b16 v[98:99], v9 offset:25680\n"
    "v_mfma_f32_16x16x32_bf16 v[24:27], v[112:115], a[64:67], v[24:27]\n"
    "ds_read_b64_tr_b16 v[100:101], v9 offset:25616\n"
    "v_mfma_f32_16x16x32_bf16 v[28:31], v[112:115], a[68:71], v[28:31]\n"
    "ds_read_b64_tr_b16 v[102:103], v9 offset:25696\n"
    "v_mfma_f32_16x16x32_bf16 v[32:35], v[116:119], a[64:67], v[32:35]\n"
    "ds_read_b64_tr_b16 v[104:105], v10 offset:25600\n"
    "v_mfma_f32_16x16x32_bf16 v[36:39], v[116:119], a[68:71], v[36:39]\n"
    "ds_read_b64_tr_b16 v[106:107], v10 offset:25680\n"
    "s_waitcnt lgkmcnt(6)\n"
    "v_mfma_f32_16x16x32_bf16 v[16:19], v[84:87], a[72:75], v[16:19]\n"
    "ds_read_b64_tr_b16 v[108:109], v9 offset:28160\n"
    "v_mfma_f32_16x16x32_bf16 v[20:23], v[84:87], a[76:79], v[20:23]\n"
    "ds_read_b64_tr_b16 v[110:111], v9 offset:28240\n"
    "v_mfma_f32_16x16x32_bf16 v[24:27], v[88:91], a[72:75], v[24:27]\n"
    "ds_read_b64_tr_b16 v[112:113], v9 offset:28176\n"
    "v_mfma_f32_16x16x32_bf16 v[28:31], v[88:91], a[76:79], v[28:31]\n"
    "ds_read_b64_tr_b16 v[114:115], v9 offset:28256\n"
    "v_mfma_f32_16x16x32_bf16 v[32:35], v[92:95], a[72:75], v[32:35]\n"
    "ds_read_b64_tr_b16 v[116:117], v10 offset:28160\n"
    "v_mfma_f32_16x16x32_bf16 v[36:39], v[92:95], a[76:79], v[36:39]\n"
    "ds_read_b64_tr_b16 v[118:119], v10 offset:28240\n"
    "s_waitcnt lgkmcnt(6)\n"
    "v_mfma_f32_16x16x32_bf16 v[16:19], v[96:99], a[80:83], v[16:19]\n"
    "ds_read_b64_tr_b16 v[84:85], v9 offset:30720\n"
    "v_mfma_f32_16x16x32_bf16 v[20:23], v[96:99], a[84:87], v[20:23]\n"
    "ds_read_b64_tr_b16 v[86:87], v9 offset:30800\n"
    "v_mfma_f32_16x16x32_bf16 v[24:27], v[100:103], a[80:83], v[24:27]\n"
    "ds_read_b64_tr_b16 v[88:89], v9 offset:30736\n"
    "v_mfma_f32_16x16x32_bf16 v[28:31], v[100:103], a[84:87], v[28:31]\n"
    "ds_read_b64_tr_b16 v[90:91], v9 offset:30816\n"
    "v_mfma_f32_16x16x32_bf16 v[32:35], v[104:107], a[80:83], v[32:35]\n"
    "ds_read_b64_tr_b16 v[92:93], v10 offset:30720\n"
    "v_mfma_f32_16x16x32_bf16 v[36:39], v[104:107], a[84:87], v[36:39]\n"
    "ds_read_b64_tr_b16 v[94:95], v10 offset:30800\n"
    "s_waitcnt lgkmcnt(6)\n"
    "v_mfma_f32_16x16x32_bf16 v[16:19], v[108:111], a[88:91], v[16:19]\n"
    "ds_read_b64_tr_b16 v[96:97], v9 offset:33280\n"
    "v_mfma_f32_16x16x32_bf16 v[20:23], v[108:111], a[92:95], v[20:23]\n"
    "ds_read_b64_tr_b16 v[98:99], v9 offset:33360\n"
    "v_mfma_f32_16x16x32_bf16 v[24:27], v[112:115], a[88:91], v[24:27]\n"
    "ds_read_b64_tr_b16 v[100:101], v9 offset:33296\n"
    "v_mfma_f32_16x16x32_bf16 v[28:31], v[112:115], a[92:95], v[28:31]\n"
    "ds_read_b64_tr_b16 v[102:103], v9 offset:33376\n"
    "v_mfma_f32_16x16x32_bf16 v[32:35], v[116:119], a[88:91], v[32:35]\n"
    "ds_read_b64_tr_b16 v[104:105], v10 offset:33280\n"
    "v_mfma_f32_16x16x32_bf16 v[36:39], v[116:119], a[92:95], v[36:39]\n"
    "ds_read_b64_tr_b16 v[106:107], v10 offset:33360\n"
    "s_waitcnt lgkmcnt(6)\n"
    "v_mfma_f32_16x16x32_bf16 v[16:19], v[84:87], a[96:99], v[16:19]\n"
    "ds_read_b64_tr_b16 v[108:109], v9 offset:35840\n"
    "v_mfma_f32_16x16x32_bf16 v[20:23], v[84:87], a[100:103], v[20:23]\n"
    "ds_read_b64_tr_b16 v[110:111], v9 offset:35920\n"
    "v_mfma_f32_16x16x32_bf16 v[24:27], v[88:91], a[96:99], v[24:27]\n"
    "ds_read_b64_tr_b16 v[112:113], v9 offset:35856\n"
    "v_mfma_f32_16x16x32_bf16 v[28:31], v[88:91], a[100:103], v[28:31]\n"
    "ds_read_b64_tr_b16 v[114:115], v9 offset:35936\n"
    "v_mfma_f32_16x16x32_bf16 v[32:35], v[92:95], a[96:99], v[32:35]\n"
    "ds_read_b64_tr_b16 v[116:117], v10 offset:35840\n"
    "v_mfma_f32_16x16x32_bf16 v[36:39], v[92:95], a[100:103], v[36:39]\n"
    "ds_read_b64_tr_b16 v[118:119], v10 offset:35920\n"
    "s_waitcnt lgkmcnt(6)\n"
    "v_mfma_f32_16x16x32_bf16 v[16:19], v[96:99], a[104:107], v[16:19]\n"
    "ds_read_b64_tr_b16 v[84:85], v9 offset:38400\n"
    "v_mfma_f32_16x16x32_bf16 v[20:23], v[96:99], a[108:111], v[20:23]\n"
    "ds_read_b64_tr_b16 v[86:87], v9 offset:38480\n"
    "v_mfma_f32_16x16x32_bf16 v[24:27], v[100:103], a[104:107], v[24:27]\n"
    "ds_read_b64_tr_b16 v[88:89], v9 offset:38416\n"
    "v_mfma_f32_16x16x32_bf16 v[28:31], v[100:103], a[108:111], v[28:31]\n"
    "ds_read_b64_tr_b16 v[90:91], v9 offset:38496\n"
    "v_mfma_f32_16x16x32_bf16 v[32:35], v[104:107], a[104:107], v[32:35]\n"
    "ds_read_b64_tr_b16 v[92:93], v10 offset:38400\n"
    "v_mfma_f32_16x16x32_bf16 v[36:39], v[104:107], a[108:111], v[36:39]\n"
    "ds_read_b64_tr_b16 v[94:95], v10 offset:38480\n"
    "s_waitcnt lgkmcnt(6)\n"
    "v_mfma_f32_16x16x32_bf16 v[16:19], v[108:111], a[112:115], v[16:19]\n"
    "ds_read_b64_tr_b16 v[96:97], v9 offset:40960\n"
    "v_mfma_f32_16x16x32_bf16 v[20:23], v[108:111], a[116:119], v[20:23]\n"
    "ds_read_b64_tr_b16 v[98:99], v9 offset:41040\n"
    "v_mfma_f32_16x16x32_bf16 v[24:27], v[112:115], a[112:115], v[24:27]\n"
    "ds_read_b64_tr_b16 v[100:101], v9 offset:40976\n"
    "v_mfma_f32_16x16x32_bf16 v[28:31], v[112:115], a[116:119], v[28:31]\n"
    "ds_read_b64_tr_b16 v[102:103], v9 offset:41056\n"
    "v_mfma_f32_16x16x32_bf16 v[32:35], v[116:119], a[112:115], v[32:35]\n"
    "ds_read_b64_tr_b16 v[104:105], v10 offset:40960\n"
    "v_mfma_f32_16x16x32_bf16 v[36:39], v[116:119], a[116:119], v[36:39]\n"
    "ds_read_b64_tr_b16 v[106:107], v10 offset:41040\n"
    "s_waitcnt lgkmcnt(6)\n"
    "v_mfma_f32_16x16x32_bf16 v[16:19], v[84:87], a[120:123], v[16:19]\n"
    "ds_read_b64_tr_b16 v[108:109], v9 offset:43520\n"
    "v_mfma_f32_16x16x32_bf16 v[20:23], v[84:87], a[124:127], v[20:23]\n"
    "ds_read_b64_tr_b16 v[110:111], v9 offset:43600\n"
    "v_mfma_f32_16x16x32_bf16 v[24:27], v[88:91], a[120:123], v[24:27]\n"
    "ds_read_b64_tr_b16 v[112:113], v9 offset:43536\n"
    "v_mfma_f32_16x16x32_bf16 v[28:31], v[88:91], a[124:127], v[28:31]\n"
    "ds_read_b64_tr_b16 v[114:115], v9 offset:43616\n"
    "v_mfma_f32_16x16x32_bf16 v[32:35], v[92:95], a[120:123], v[32:35]\n"
    "ds_read_b64_tr_b16 v[116:117], v10 offset:43520\n"
    "v_mfma_f32_16x16x32_bf16 v[36:39], v[92:95], a[124:127], v[36:39]\n"
    "ds_read_b64_tr_b16 v[118:119], v10 offset:43600\n"
    "s_waitcnt lgkmcnt(6)\n"
    "v_mfma_f32_16x16x32_bf16 v[16:19], v[96:99], v[52:55], v[16:19]\n"
    "ds_read_b64_tr_b16 v[84:85], v9 offset:46080\n"
    "v_mfma_f32_16x16x32_bf16 v[20:23], v[96:99], v[56:59], v[20:23]\n"
    "ds_read_b64_tr_b16 v[86:87], v9 offset:46160\n"
    "v_mfma_f32_16x16x32_bf16 v[24:27], v[100:103], v[52:55], v[24:27]\n"
    "ds_read_b64_tr_b16 v[88:89], v9 offset:46096\n"
    "v_mfma_f32_16x16x32_bf16 v[28:31], v[100:103], v[56:59], v[28:31]\n"
    "ds_read_b64_tr_b16 v[90:91], v9 offset:46176\n"
    "v_mfma_f32_16x16x32_bf16 v[32:35], v[104:107], v[52:55], v[32:35]\n"
    "ds_read_b64_tr_b16 v[92:93], v10 offset:46080\n"
    "v_mfma_f32_16x16x32_bf16 v[36:39], v[104:107], v[56:59], v[36:39]\n"
    "ds_read_b64_tr_b16 v[94:95], v10 offset:46160\n"
    "s_waitcnt lgkmcnt(6)\n"
    "v_mfma_f32_16x16x32_bf16 v[16:19], v[108:111], v[60:63], v[16:19]\n"
    "ds_read_b64_tr_b16 v[96:97], v9 offset:48640\n"
    "v_mfma_f32_16x16x32_bf16 v[20:23], v[108:111], v[64:67], v[20:23]\n"
    "ds_read_b64_tr_b16 v[98:99], v9 offset:48720\n"
    "v_mfma_f32_16x16x32_bf16 v[24:27], v[112:115], v[60:63], v[24:27]\n"
    "ds_read_b64_tr_b16 v[100:101], v9 offset:48656\n"
    "v_mfma_f32_16x16x32_bf16 v[28:31], v[112:115], v[64:67], v[28:31]\n"
    "ds_read_b64_tr_b16 v[102:103], v9 offset:48736\n"
    "v_mfma_f32_16x16x32_bf16 v[32:35], v[116:119], v[60:63], v[32:35]\n"
    "ds_read_b64_tr_b16 v[104:105], v10 offset:48640\n"
    "v_mfma_f32_16x16x32_bf16 v[36:39], v[116:119], v[64:67], v[36:39]\n"
    "ds_read_b64_tr_b16 v[106:107], v10 offset:48720\n"
    "s_waitcnt lgkmcnt(6)\n"
    "v_mfma_f32_16x16x32_bf16 v[16:19], v[84:87], v[68:71], v[16:19]\n"
    "ds_read_b64_tr_b16 a[0:1], v124 offset:0\n"
    "ds_read_b64_tr_b16 a[12:13], v126 offset:0\n"
    "v_mfma_f32_16x16x32_bf16 v[20:23], v[84:87], v[72:75], v[20:23]\n"
    "ds_read_b64_tr_b16 a[2:3], v124 offset:160\n"
    "ds_read_b64_tr_b16 a[14:15], v126 offset:160\n"
    "v_mfma_f32_16x16x32_bf16 v[24:27], v[88:91], v[68:71], v[24:27]\n"
    "ds_read_b64_tr_b16 a[4:5], v124 offset:320\n"
    "ds_read_b64_tr_b16 a[16:17], v126 offset:320\n"
    "v_mfma_f32_16x16x32_bf16 v[28:31], v[88:91], v[72:75], v[28:31]\n"
    "ds_read_b64_tr_b16 a[6:7], v124 offset:480\n"
    "ds_read_b64_tr_b16 a[18:19], v126 offset:480\n"
    "v_mfma_f32_16x16x32_bf16 v[32:35], v[92:95], v[68:71], v[32:35]\n"
    "ds_read_b64_tr_b16 a[8:9], v125 offset:0\n"
    "ds_read_b64_tr_b16 a[20:21], v127 offset:0\n"
    "v_mfma_f32_16x16x32_bf16 v[36:39], v[92:95], v[72:75], v[36:39]\n"
    "s_waitcnt lgkmcnt(10)\n"
    "v_mfma_f32_16x16x32_bf16 v[16:19], v[96:99], v[76:79], v[16:19]\n"
    "v_mfma_f32_16x16x32_bf16 v[20:23], v[96:99], v[80:83], v[20:23]\n"
    "v_mfma_f32_16x16x32_bf16 v[24:27], v[100:103], v[76:79], v[24:27]\n"
    "v_mfma_f32_16x16x32_bf16 v[28:31], v[100:103], v[80:83], v[28:31]\n"
    "v_mfma_f32_16x16x32_bf16 v[32:35], v[104:107], v[76:79], v[32:35]\n"
    "v_mfma_f32_16x16x32_bf16 v[36:39], v[104:107], v[80:83], v[36:39]\n"
    "s_nop 2\n"
    "v_cvt_pk_bf16_f32 v40, v16, v17\n"
    "v_cvt_pk_bf16_f32 v42, v20, v21\n"
    "v_cvt_pk_bf16_f32 v41, v18, v19\n"
    "v_cvt_pk_bf16_f32 v43, v22, v23\n"
    "v_cvt_pk_bf16_f32 v44, v24, v25\n"
    "v_cvt_pk_bf16_f32 v46, v28, v29\n"
    "v_cvt_pk_bf16_f32 v45, v26, v27\n"
    "v_permlane16_swap_b32 v40, v42\n"
    "v_cvt_pk_bf16_f32 v47, v30, v31\n"
    "v_cvt_pk_bf16_f32 v48, v32, v33\n"
    "v_permlane16_swap_b32 v41, v43\n"
    "v_cvt_pk_bf16_f32 v50, v36, v37\n"
    "v_cvt_pk_bf16_f32 v49, v34, v35\n"
    "v_permlane16_swap_b32 v44, v46\n"
    "v_cvt_pk_bf16_f32 v51, v38, v39\n"
    "v_permlane16_swap_b32 v45, v47\n"
    "v_permlane16_swap_b32 v48, v50\n"
    "v_permlane16_swap_b32 v49, v51\n"
    "s_waitcnt lgkmcnt(7)\n"
    "v_mfma_f32_32x32x16_bf16 v[84:99], a[0:3], v[40:43], 0\n"
    "s_waitcnt lgkmcnt(6)\n"
    "v_mfma_f32_32x32x16_bf16 v[100:115], a[12:15], v[40:43], 0\n"
    "s_waitcnt lgkmcnt(3)\n"
    "v_mfma_f32_32x32x16_bf16 v[84:99], a[4:7], v[44:47], v[84:99]\n"
    "s_waitcnt lgkmcnt(2)\n"
    "v_mfma_f32_32x32x16_bf16 v[100:115], a[16:19], v[44:47], v[100:115]\n"
    "s_waitcnt lgkmcnt(1)\n"
    "v_mfma_f32_32x32x16_bf16 v[84:99], a[8:11], v[48:51], v[84:99]\n"
    "s_waitcnt lgkmcnt(0)\n"
    "v_mfma_f32_32x32x16_bf16 v[100:115], a[20:23], v[48:51], v[100:115]\n"
    "s_nop 11\n"
    "v_max3_f32 v104, v84, v85, v86\n"
    "v_min3_f32 v106, v84, v85, v86\n"
    "v_max3_f32 v104, v104, v87, v88\n"
    "v_min3_f32 v106, v106, v87, v88\n"
    "v_max3_f32 v104, v104, v89, v90\n"
    "v_min3_f32 v106, v106, v89, v90\n"
    "v_max3_f32 v104, v104, v91, v92\n"
    "v_min3_f32 v106, v106, v91, v92\n"
    "v_max3_f32 v104, v104, v93, v94\n"
    "v_min3_f32 v106, v106, v93, v94\n"
    "v_max3_f32 v104, v104, v95, v96\n"
    "v_min3_f32 v106, v106, v95, v96\n"
    "v_max3_f32 v104, v104, v97, v98\n"
    "v_min3_f32 v106, v106, v97, v98\n"
    "v_max3_f32 v104, v104, v99, v100\n"
    "v_min3_f32 v106, v106, v99, v100\n"
    "v_max3_f32 v104, v104, v101, v102\n"
    "v_min3_f32 v106, v106, v101, v102\n"
    "v_max_f32 v104, v104, v103\n"
    "v_min_f32 v106, v106, v103\n"
    "v_mul_f32_e64 v104, -v104, s78\n"
    "v_mul_f32 v106, s78, v106\n"
    "v_pk_fma_f32 v[16:17], v[84:85], s[78:79], v[104:105] op_sel_hi:[1,0,0]\n"
    "v_pk_fma_f32 v[84:85], v[84:85], s[78:79], v[106:107] op_sel_hi:[1,0,0] neg_lo:[0,1,0] neg_hi:[0,1,0]\n"
    "v_pk_fma_f32 v[18:19], v[86:87], s[78:79], v[104:105] op_sel_hi:[1,0,0]\n"
    "v_pk_fma_f32 v[86:87], v[86:87], s[78:79], v[106:107] op_sel_hi:[1,0,0] neg_lo:[0,1,0] neg_hi:[0,1,0]\n"
    "v_pk_fma_f32 v[20:21], v[88:89], s[78:79], v[104:105] op_sel_hi:[1,0,0]\n"
    "v_pk_fma_f32 v[88:89], v[88:89], s[78:79], v[106:107] op_sel_hi:[1,0,0] neg_lo:[0,1,0] neg_hi:[0,1,0]\n"
    "v_pk_fma_f32 v[22:23], v[90:91], s[78:79], v[104:105] op_sel_hi:[1,0,0]\n"
    "v_pk_fma_f32 v[90:91], v[90:91], s[78:79], v[106:107] op_sel_hi:[1,0,0] neg_lo:[0,1,0] neg_hi:[0,1,0]\n"
    "v_pk_fma_f32 v[24:25], v[92:93], s[78:79], v[104:105] op_sel_hi:[1,0,0]\n"
    "v_pk_fma_f32 v[92:93], v[92:93], s[78:79], v[106:107] op_sel_hi:[1,0,0] neg_lo:[0,1,0] neg_hi:[0,1,0]\n"
    "v_pk_fma_f32 v[26:27], v[94:95], s[78:79], v[104:105] op_sel_hi:[1,0,0]\n"
    "v_pk_fma_f32 v[94:95], v[94:95], s[78:79], v[106:107] op_sel_hi:[1,0,0] neg_lo:[0,1,0] neg_hi:[0,1,0]\n"
    "v_pk_fma_f32 v[28:29], v[96:97], s[78:79], v[104:105] op_sel_hi:[1,0,0]\n"
    "v_pk_fma_f32 v[96:97], v[96:97], s[78:79], v[106:107] op_sel_hi:[1,0,0] neg_lo:[0,1,0] neg_hi:[0,1,0]\n"
    "v_pk_fma_f32 v[30:31], v[98:99], s[78:79], v[104:105] op_sel_hi:[1,0,0]\n"
    "v_pk_fma_f32 v[98:99], v[98:99], s[78:79], v[106:107] op_sel_hi:[1,0,0] neg_lo:[0,1,0] neg_hi:[0,1,0]\n"
    "v_pk_fma_f32 v[32:33], v[100:101], s[78:79], v[104:105] op_sel_hi:[1,0,0]\n"
    "v_pk_fma_f32 v[100:101], v[100:101], s[78:79], v[106:107] op_sel_hi:[1,0,0] neg_lo:[0,1,0] neg_hi:[0,1,0]\n"
    "v_pk_fma_f32 v[34:35], v[102:103], s[78:79], v[104:105] op_sel_hi:[1,0,0]\n"
    "v_pk_fma_f32 v[102:103], v[102:103], s[78:79], v[106:107] op_sel_hi:[1,0,0] neg_lo:[0,1,0] neg_hi:[0,1,0]\n"
    "v_exp_f32 v16, v16\n"
    "v_exp_f32 v17, v17\n"
    "v_exp_f32 v84, v84\n"
    "v_exp_f32 v85, v85\n"
    "v_exp_f32 v18, v18\n"
    "v_exp_f32 v19, v19\n"
    "v_exp_f32 v86, v86\n"
    "v_exp_f32 v87, v87\n"
    "v_exp_f32 v20, v20\n"
    "v_exp_f32 v21, v21\n"
    "v_exp_f32 v88, v88\n"
    "v_exp_f32 v89, v89\n"
    "v_pk_add_f32 v[108:109], v[16:17], v[18:19]\n"
    "v_pk_add_f32 v[110:111], v[84:85], v[86:87]\n"
    "v_exp_f32 v22, v22\n"
    "v_exp_f32 v23, v23\n"
    "v_exp_f32 v90, v90\n"
    "v_exp_f32 v91, v91\n"
    "v_pk_add_f32 v[108:109], v[108:109], v[20:21]\n"
    "v_pk_add_f32 v[110:111], v[110:111], v[88:89]\n"
    "v_exp_f32 v24, v24\n"
    "v_exp_f32 v25, v25\n"
    "v_exp_f32 v92, v92\n"
    "v_exp_f32 v93, v93\n"
    "v_pk_add_f32 v[108:109], v[108:109], v[22:23]\n"
    "v_pk_add_f32 v[110:111], v[110:111], v[90:91]\n"
    "v_exp_f32 v26, v26\n"
    "v_exp_f32 v27, v27\n"
    "v_exp_f32 v94, v94\n"
    "v_exp_f32 v95, v95\n"
    "v_pk_add_f32 v[108:109], v[108:109], v[24:25]\n"
    "v_pk_add_f32 v[110:111], v[110:111], v[92:93]\n"
    "v_exp_f32 v28, v28\n"
    "v_exp_f32 v29, v29\n"
    "v_exp_f32 v96, v96\n"
    "v_exp_f32 v97, v97\n"
    "v_pk_add_f32 v[108:109], v[108:109], v[26:27]\n"
    "v_pk_add_f32 v[110:111], v[110:111], v[94:95]\n"
    "v_exp_f32 v30, v30\n"
    "v_exp_f32 v31, v31\n"
    "v_exp_f32 v98, v98\n"
    "v_exp_f32 v99, v99\n"
    "v_pk_add_f32 v[108:109], v[108:109], v[28:29]\n"
    "v_pk_add_f32 v[110:111], v[110:111], v[96:97]\n"
    "v_exp_f32 v32, v32\n"
    "v_exp_f32 v33, v33\n"
    "v_exp_f32 v100, v100\n"
    "v_exp_f32 v101, v101\n"
    "v_pk_add_f32 v[108:109], v[108:109], v[30:31]\n"
    "v_pk_add_f32 v[110:111], v[110:111], v[98:99]\n"
    "v_exp_f32 v34, v34\n"
    "v_exp_f32 v35, v35\n"
    "v_exp_f32 v102, v102\n"
    "v_exp_f32 v103, v103\n"
    "v_pk_add_f32 v[108:109], v[108:109], v[32:33]\n"
    "v_pk_add_f32 v[110:111], v[110:111], v[100:101]\n"
    "v_pk_add_f32 v[108:109], v[108:109], v[34:35]\n"
    "v_pk_add_f32 v[110:111], v[110:111], v[102:103]\n"
    "v_add_f32 v108, v108, v109\n"
    "v_add_f32 v110, v110, v111\n"
    "v_rcp_f32 v108, v108\n"
    "v_rcp_f32 v110, v110\n"
    "v_pk_mul_f32 v[16:17], v[16:17], v[108:109] op_sel_hi:[1,0]\n"
    "v_pk_mul_f32 v[18:19], v[18:19], v[108:109] op_sel_hi:[1,0]\n"
    "v_pk_mul_f32 v[20:21], v[20:21], v[108:109] op_sel_hi:[1,0]\n"
    "v_pk_mul_f32 v[22:23], v[22:23], v[108:109] op_sel_hi:[1,0]\n"
    "v_pk_mul_f32 v[24:25], v[24:25], v[108:109] op_sel_hi:[1,0]\n"
    "v_pk_mul_f32 v[26:27], v[26:27], v[108:109] op_sel_hi:[1,0]\n"
    "v_pk_mul_f32 v[28:29], v[28:29], v[108:109] op_sel_hi:[1,0]\n"
    "v_pk_mul_f32 v[30:31], v[30:31], v[108:109] op_sel_hi:[1,0]\n"
    "v_pk_mul_f32 v[32:33], v[32:33], v[108:109] op_sel_hi:[1,0]\n"
    "v_pk_mul_f32 v[34:35], v[34:35], v[108:109] op_sel_hi:[1,0]\n"
    "v_pk_mul_f32 v[84:85], v[84:85], v[110:111] op_sel_hi:[1,0]\n"
    "v_pk_mul_f32 v[86:87], v[86:87], v[110:111] op_sel_hi:[1,0]\n"
    "v_pk_mul_f32 v[88:89], v[88:89], v[110:111] op_sel_hi:[1,0]\n"
    "v_pk_mul_f32 v[90:91], v[90:91], v[110:111] op_sel_hi:[1,0]\n"
    "v_pk_mul_f32 v[92:93], v[92:93], v[110:111] op_sel_hi:[1,0]\n"
    "v_pk_mul_f32 v[94:95], v[94:95], v[110:111] op_sel_hi:[1,0]\n"
    "v_pk_mul_f32 v[96:97], v[96:97], v[110:111] op_sel_hi:[1,0]\n"
    "v_pk_mul_f32 v[98:99], v[98:99], v[110:111] op_sel_hi:[1,0]\n"
    "v_pk_mul_f32 v[100:101], v[100:101], v[110:111] op_sel_hi:[1,0]\n"
    "v_pk_mul_f32 v[102:103], v[102:103], v[110:111] op_sel_hi:[1,0]\n"
    "v_cvt_pk_bf16_f32 v16, v16, v17\n"
    "v_cvt_pk_bf16_f32 v17, v18, v19\n"
    "v_cvt_pk_bf16_f32 v18, v20, v21\n"
    "v_cvt_pk_bf16_f32 v19, v22, v23\n"
    "v_cvt_pk_bf16_f32 v20, v24, v25\n"
    "v_cvt_pk_bf16_f32 v21, v26, v27\n"
    "v_cvt_pk_bf16_f32 v22, v28, v29\n"
    "v_cvt_pk_bf16_f32 v23, v30, v31\n"
    "v_cvt_pk_bf16_f32 v24, v32, v33\n"
    "v_cvt_pk_bf16_f32 v25, v34, v35\n"
    "v_cvt_pk_bf16_f32 v26, v84, v85\n"
    "v_cvt_pk_bf16_f32 v27, v86, v87\n"
    "v_cvt_pk_bf16_f32 v28, v88, v89\n"
    "v_cvt_pk_bf16_f32 v29, v90, v91\n"
    "v_cvt_pk_bf16_f32 v30, v92, v93\n"
    "v_cvt_pk_bf16_f32 v31, v94, v95\n"
    "v_cvt_pk_bf16_f32 v32, v96, v97\n"
    "v_cvt_pk_bf16_f32 v33, v98, v99\n"
    "v_cvt_pk_bf16_f32 v34, v100, v101\n"
    "v_cvt_pk_bf16_f32 v35, v102, v103\n"
    "s_barrier\n"
    "ds_read_b128 a[24:27], v122 offset:0\n"
    "ds_read_b128 a[28:31], v122 offset:16\n"
    "ds_read_b128 a[32:35], v122 offset:32\n"
    "ds_read_b128 a[36:39], v122 offset:48\n"
    "ds_read_b128 a[40:43], v122 offset:64\n"
    "ds_read_b128 a[44:47], v121 offset:0\n"
    "ds_read_b128 a[48:51], v121 offset:16\n"
    "ds_read_b128 a[52:55], v121 offset:32\n"
    "ds_read_b128 a[56:59], v121 offset:48\n"
    "ds_read_b128 a[60:63], v121 offset:64\n"
    "s_waitcnt lgkmcnt(9)\n"
    "v_mfma_f32_32x32x16_bf16 v[100:115], a[24:27], v[16:19], 0\n"
    "s_waitcnt lgkmcnt(8)\n"
    "v_mfma_f32_32x32x16_bf16 v[100:115], a[28:31], v[20:23], v[100:115]\n"
    "s_waitcnt lgkmcnt(7)\n"
    "v_mfma_f32_32x32x16_bf16 v[100:115], a[32:35], v[24:27], v[100:115]\n"
    "s_waitcnt lgkmcnt(6)\n"
    "v_mfma_f32_32x32x16_bf16 v[100:115], a[36:39], v[28:31], v[100:115]\n"
    "s_waitcnt lgkmcnt(5)\n"
    "v_mfma_f32_32x32x16_bf16 v[100:115], a[40:43], v[32:35], v[100:115]\n"
    "s_waitcnt lgkmcnt(4)\n"
    "v_mfma_f32_32x32x16_bf16 v[84:99], a[44:47], v[16:19], 0\n"
    "s_waitcnt lgkmcnt(3)\n"
    "v_mfma_f32_32x32x16_bf16 v[84:99], a[48:51], v[20:23], v[84:99]\n"
    "s_waitcnt lgkmcnt(2)\n"
    "v_mfma_f32_32x32x16_bf16 v[84:99], a[52:55], v[24:27], v[84:99]\n"
    "s_waitcnt lgkmcnt(1)\n"
    "v_mfma_f32_32x32x16_bf16 v[84:99], a[56:59], v[28:31], v[84:99]\n"
    "s_waitcnt lgkmcnt(0)\n"
    "v_mfma_f32_32x32x16_bf16 v[84:99], a[60:63], v[32:35], v[84:99]\n"
    "s_nop 1\n"
    "v_add_f32 v36, 0x358637bd, v104\n"
    "v_rcp_f32 v36, v36\n"
    "s_nop 0\n"
    "v_mul_f32 v100, v100, v36\n"
    "v_mul_f32 v101, v101, v36\n"
    "v_mul_f32 v102, v102, v36\n"
    "v_mul_f32 v103, v103, v36\n"
    "v_cvt_pk_bf16_f32 v48, v100, v101\n"
    "v_cvt_pk_bf16_f32 v49, v102, v103\n"
    "v_min_u32 v48, -2, v48\n"
    "v_min_u32 v49, -2, v49\n"
    "buffer_store_dwordx2 v[48:49], v6, s[36:39], s86 offen offset:512 sc1\n"
    "v_pk_mul_f32 v[84:85], v[84:85], v[36:37] op_sel_hi:[1,0]\n"
    "v_pk_mul_f32 v[86:87], v[86:87], v[36:37] op_sel_hi:[1,0]\n"
    "v_pk_mul_f32 v[88:89], v[88:89], v[36:37] op_sel_hi:[1,0]\n"
    "v_pk_mul_f32 v[90:91], v[90:91], v[36:37] op_sel_hi:[1,0]\n"
    "v_cvt_pk_bf16_f32 v40, v84, v85\n"
    "v_cvt_pk_bf16_f32 v41, v86, v87\n"
    "v_cvt_pk_bf16_f32 v42, v88, v89\n"
    "v_cvt_pk_bf16_f32 v43, v90, v91\n"
    "v_min_u32 v40, -2, v40\n"
    "v_min_u32 v41, -2, v41\n"
    "v_min_u32 v42, -2, v42\n"
    "v_min_u32 v43, -2, v43\n"
    "buffer_store_dwordx4 v[40:43], v123, s[36:39], s86 offen offset:0 sc1\n"
    "v_pk_mul_f32 v[92:93], v[92:93], v[36:37] op_sel_hi:[1,0]\n"
    "v_pk_mul_f32 v[94:95], v[94:95], v[36:37] op_sel_hi:[1,0]\n"
    "v_pk_mul_f32 v[96:97], v[96:97], v[36:37] op_sel_hi:[1,0]\n"
    "v_pk_mul_f32 v[98:99], v[98:99], v[36:37] op_sel_hi:[1,0]\n"
    "v_cvt_pk_bf16_f32 v44, v92, v93\n"
    "v_cvt_pk_bf16_f32 v45, v94, v95\n"
    "v_cvt_pk_bf16_f32 v46, v96, v97\n"
    "v_cvt_pk_bf16_f32 v47, v98, v99\n"
    "v_min_u32 v44, -2, v44\n"
    "v_min_u32 v45, -2, v45\n"
    "v_min_u32 v46, -2, v46\n"
    "v_min_u32 v47, -2, v47\n"
    "buffer_store_dwordx4 v[44:47], v123, s[36:39], s86 offen offset:256 sc1\n"
    "s_setprio 0\n"
    "s_barrier\n"
    "v_bfe_u32 v11, v1, 2, 2\n"
    "v_bfe_u32 v12, v1, 4, 1\n"
    "v_lshrrev_b32 v13, 5, v1\n"
    "v_lshl_or_b32 v11, v12, 2, v11\n"
    "v_lshl_or_b32 v11, v13, 4, v11\n"
    "v_mul_u32_u24 v11, 160, v11\n"
    "v_and_b32 v2, 3, v1\n"
    "v_lshl_add_u32 v2, v2, 3, v11\n"
    "v_add_u32 v2, 0, v2\n"
    "v_and_b32 v11, 15, v1\n"
    "v_mul_u32_u24 v11, 0x500, v11\n"
    "v_lshl_add_u32 v3, v12, 5, v11\n"
    "v_lshl_add_u32 v3, v13, 4, v3\n"
    "v_lshrrev_b32 v12, 4, v1\n"
    "v_lshl_add_u32 v4, v12, 3, v11\n"
    "s_mul_i32 s15, s4, 5120\n"
    "s_add_u32 s15, s15, 102400\n"
    "v_add_u32 v5, s15, v14\n"
    "s_lshl_b32 s15, s10, 2\n"
    "s_add_u32 s15, s15, s4\n"
    "s_mul_i32 s14, s15, 20480\n"
    "s_lshl_b32 s65, s12, 10\n"
    "s_add_u32 s16, s65, 1024\n"
    "s_cmp_eq_u32 s16, 10240\n"
    "s_cselect_b32 s66, 0, s16\n"
    "s_add_u32 s16, s66, 1024\n"
    "s_cmp_eq_u32 s16, 10240\n"
    "s_cselect_b32 s67, 0, s16\n"
    "s_add_u32 s16, s67, 1024\n"
    "s_cmp_eq_u32 s16, 10240\n"
    "s_cselect_b32 s68, 0, s16\n"
    "s_add_u32 s16, s68, 1024\n"
    "s_cmp_eq_u32 s16, 10240\n"
    "s_cselect_b32 s69, 0, s16\n"
    "s_add_u32 s16, s69, 1024\n"
    "s_cmp_eq_u32 s16, 10240\n"
    "s_cselect_b32 s70, 0, s16\n"
    "s_add_u32 s16, s70, 1024\n"
    "s_cmp_eq_u32 s16, 10240\n"
    "s_cselect_b32 s71, 0, s16\n"
    "s_add_u32 s16, s71, 1024\n"
    "s_cmp_eq_u32 s16, 10240\n"
    "s_cselect_b32 s72, 0, s16\n"
    "s_add_u32 s16, s72, 1024\n"
    "s_cmp_eq_u32 s16, 10240\n"
    "s_cselect_b32 s73, 0, s16\n"
    "s_add_u32 s16, s73, 1024\n"
    "s_cmp_eq_u32 s16, 10240\n"
    "s_cselect_b32 s74, 0, s16\n"
    "s_add_u32 s15, s14, 911360\n"
    "s_add_u32 s65, s65, s15\n"
    "s_add_u32 s66, s66, s15\n"
    "s_add_u32 s67, s67, s15\n"
    "s_add_u32 s68, s68, s15\n"
    "s_add_u32 s69, s69, s15\n"
    "s_add_u32 s70, s70, s15\n"
    "s_add_u32 s71, s71, s15\n"
    "s_add_u32 s72, s72, s15\n"
    "s_add_u32 s73, s73, s15\n"
    "s_add_u32 s74, s74, s15\n"
    "s_mul_i32 s15, s11, 160\n"
    "s_add_u32 s14, s14, s15\n"
    "s_mov_b32 s91, 1\n"
    "s_mov_b32 s50, 2621440\n"
    "s_mov_b32 s51, 0x20000\n"
    "s_barrier\n"
    "ds_read_b64_tr_b16 a[0:1], v2 offset:0\n"
    "ds_read_b64_tr_b16 a[2:3], v2 offset:1280\n"
    "ds_read_b64_tr_b16 a[4:5], v2 offset:32\n"
    "ds_read_b64_tr_b16 a[6:7], v2 offset:1312\n"
    "ds_read_b64_tr_b16 a[8:9], v2 offset:64\n"
    "ds_read_b64_tr_b16 a[10:11], v2 offset:1344\n"
    "ds_read_b64_tr_b16 a[12:13], v2 offset:96\n"
    "ds_read_b64_tr_b16 a[14:15], v2 offset:1376\n"
    "ds_read_b64_tr_b16 a[16:17], v2 offset:128\n"
    "ds_read_b64_tr_b16 a[18:19], v2 offset:1408\n"
    "ds_read_b64_tr_b16 a[20:21], v2 offset:5120\n"
    "ds_read_b64_tr_b16 a[22:23], v2 offset:6400\n"
    "ds_read_b64_tr_b16 a[24:25], v2 offset:5152\n"
    "ds_read_b64_tr_b16 a[26:27], v2 offset:6432\n"
    "ds_read_b64_tr_b16 a[28:29], v2 offset:5184\n"
    "ds_read_b64_tr_b16 a[30:31], v2 offset:6464\n"
    "ds_read_b64_tr_b16 a[32:33], v2 offset:5216\n"
    "ds_read_b64_tr_b16 a[34:35], v2 offset:6496\n"
    "ds_read_b64_tr_b16 a[36:37], v2 offset:5248\n"
    "ds_read_b64_tr_b16 a[38:39], v2 offset:6528\n"
    "ds_read_b64_tr_b16 a[40:41], v2 offset:10240\n"
    "ds_read_b64_tr_b16 a[42:43], v2 offset:11520\n"
    "ds_read_b64_tr_b16 a[44:45], v2 offset:10272\n"
    "ds_read_b64_tr_b16 a[46:47], v2 offset:11552\n"
    "ds_read_b64_tr_b16 a[48:49], v2 offset:10304\n"
    "ds_read_b64_tr_b16 a[50:51], v2 offset:11584\n"
    "ds_read_b64_tr_b16 a[52:53], v2 offset:10336\n"
    "ds_read_b64_tr_b16 a[54:55], v2 offset:11616\n"
    "ds_read_b64_tr_b16 a[56:57], v2 offset:10368\n"
    "ds_read_b64_tr_b16 a[58:59], v2 offset:11648\n"
    "ds_read_b64_tr_b16 a[60:61], v2 offset:15360\n"
    "ds_read_b64_tr_b16 a[62:63], v2 offset:16640\n"
    "ds_read_b64_tr_b16 a[64:65], v2 offset:15392\n"
    "ds_read_b64_tr_b16 a[66:67], v2 offset:16672\n"
    "ds_read_b64_tr_b16 a[68:69], v2 offset:15424\n"
    "ds_read_b64_tr_b16 a[70:71], v2 offset:16704\n"
    "ds_read_b64_tr_b16 a[72:73], v2 offset:15456\n"
    "ds_read_b64_tr_b16 a[74:75], v2 offset:16736\n"
    "ds_read_b64_tr_b16 a[76:77], v2 offset:15488\n"
    "ds_read_b64_tr_b16 a[78:79], v2 offset:16768\n"
    "ds_read_b64_tr_b16 a[80:81], v2 offset:20480\n"
    "ds_read_b64_tr_b16 a[82:83], v2 offset:21760\n"
    "ds_read_b64_tr_b16 a[84:85], v2 offset:20512\n"
    "ds_read_b64_tr_b16 a[86:87], v2 offset:21792\n"
    "ds_read_b64_tr_b16 a[88:89], v2 offset:20544\n"
    "ds_read_b64_tr_b16 a[90:91], v2 offset:21824\n"
    "ds_read_b64_tr_b16 a[92:93], v2 offset:20576\n"
    "ds_read_b64_tr_b16 a[94:95], v2 offset:21856\n"
    "ds_read_b64_tr_b16 a[96:97], v2 offset:20608\n"
    "ds_read_b64_tr_b16 a[98:99], v2 offset:21888\n"
    "ds_read_b64_tr_b16 a[100:101], v2 offset:25600\n"
    "ds_read_b64_tr_b16 a[102:103], v2 offset:26880\n"
    "ds_read_b64_tr_b16 a[104:105], v2 offset:25632\n"
    "ds_read_b64_tr_b16 a[106:107], v2 offset:26912\n"
    "ds_read_b64_tr_b16 a[108:109], v2 offset:25664\n"
    "ds_read_b64_tr_b16 a[110:111], v2 offset:26944\n"
    "ds_read_b64_tr_b16 a[112:113], v2 offset:25696\n"
    "ds_read_b64_tr_b16 a[114:115], v2 offset:26976\n"
    "ds_read_b64_tr_b16 a[116:117], v2 offset:25728\n"
    "ds_read_b64_tr_b16 a[118:119], v2 offset:27008\n"
    "ds_read_b64_tr_b16 a[120:121], v2 offset:30720\n"
    "ds_read_b64_tr_b16 a[122:123], v2 offset:32000\n"
    "ds_read_b64_tr_b16 a[124:125], v2 offset:30752\n"
    "ds_read_b64_tr_b16 a[126:127], v2 offset:32032\n"
    "ds_read_b64_tr_b16 v[80:81], v2 offset:30784\n"
    "ds_read_b64_tr_b16 v[82:83], v2 offset:32064\n"
    "ds_read_b64_tr_b16 v[84:85], v2 offset:30816\n"
    "ds_read_b64_tr_b16 v[86:87], v2 offset:32096\n"
    "ds_read_b64_tr_b16 v[88:89], v2 offset:30848\n"
    "ds_read_b64_tr_b16 v[90:91], v2 offset:32128\n"
    "ds_read_b64_tr_b16 v[92:93], v2 offset:35840\n"
    "ds_read_b64_tr_b16 v[94:95], v2 offset:37120\n"
    "ds_read_b64_tr_b16 v[96:97], v2 offset:35872\n"
    "ds_read_b64_tr_b16 v[98:99], v2 offset:37152\n"
    "ds_read_b64_tr_b16 v[100:101], v2 offset:35904\n"
    "ds_read_b64_tr_b16 v[102:103], v2 offset:37184\n"
    "ds_read_b64_tr_b16 v[104:105], v2 offset:35936\n"
    "ds_read_b64_tr_b16 v[106:107], v2 offset:37216\n"
    "ds_read_b64_tr_b16 v[108:109], v2 offset:35968\n"
    "ds_read_b64_tr_b16 v[110:111], v2 offset:37248\n"
    "ds_read_b64_tr_b16 v[112:113], v2 offset:40960\n"
    "ds_read_b64_tr_b16 v[114:115], v2 offset:42240\n"
    "ds_read_b64_tr_b16 v[116:117], v2 offset:40992\n"
    "ds_read_b64_tr_b16 v[118:119], v2 offset:42272\n"
    "ds_read_b64_tr_b16 v[120:121], v2 offset:41024\n"
    "ds_read_b64_tr_b16 v[122:123], v2 offset:42304\n"
    "ds_read_b64_tr_b16 v[124:125], v2 offset:41056\n"
    "ds_read_b64_tr_b16 v[126:127], v2 offset:42336\n"
    "s_waitcnt vmcnt(0)\n"
    "s_barrier\n"
    "L_G2%=:\n"
    "buffer_load_dwordx4 v[40:43], v14, s[36:39], s65 offen sc1\n"
    "buffer_load_dwordx4 v[44:47], v14, s[36:39], s66 offen sc1\n"
    "buffer_load_dwordx4 v[48:51], v14, s[36:39], s67 offen sc1\n"
    "buffer_load_dwordx4 v[52:55], v14, s[36:39], s68 offen sc1\n"
    "buffer_load_dwordx4 v[56:59], v14, s[36:39], s69 offen sc1\n"
    "buffer_load_dwordx4 v[60:63], v14, s[36:39], s70 offen sc1\n"
    "buffer_load_dwordx4 v[64:67], v14, s[36:39], s71 offen sc1\n"
    "buffer_load_dwordx4 v[68:71], v14, s[36:39], s72 offen sc1\n"
    "buffer_load_dwordx4 v[72:75], v14, s[36:39], s73 offen sc1\n"
    "buffer_load_dwordx4 v[76:79], v14, s[36:39], s74 offen sc1\n"
    "s_waitcnt lgkmcnt(0)\n"
    "s_waitcnt vmcnt(8)\n"
    "v_max3_u32 v11, v40, v41, v42\n"
    "v_max3_u32 v11, v11, v43, v44\n"
    "v_max3_u32 v11, v11, v45, v46\n"
    "v_max_u32 v11, v11, v47\n"
    "v_cmp_eq_u32 vcc, -1, v11\n"
    "s_cmp_lg_u64 vcc, 0\n"
    "s_cbranch_scc1 L_o0_slow%=\n"
    "L_o0_done%=:\n"
    "v_mfma_f32_16x16x32_bf16 v[16:19], a[0:3], v[40:43], 0\n"
    "v_mfma_f32_16x16x32_bf16 v[20:23], a[4:7], v[40:43], 0\n"
    "v_mfma_f32_16x16x32_bf16 v[24:27], a[8:11], v[40:43], 0\n"
    "v_mfma_f32_16x16x32_bf16 v[28:31], a[12:15], v[40:43], 0\n"
    "v_mfma_f32_16x16x32_bf16 v[32:35], a[16:19], v[40:43], 0\n"
    "s_waitcnt vmcnt(6)\n"
    "v_mfma_f32_16x16x32_bf16 v[16:19], a[20:23], v[44:47], v[16:19]\n"
    "v_max3_u32 v11, v48, v49, v50\n"
    "v_mfma_f32_16x16x32_bf16 v[20:23], a[24:27], v[44:47], v[20:23]\n"
    "v_max3_u32 v11, v11, v51, v52\n"
    "v_mfma_f32_16x16x32_bf16 v[24:27], a[28:31], v[44:47], v[24:27]\n"
    "v_max3_u32 v11, v11, v53, v54\n"
    "v_mfma_f32_16x16x32_bf16 v[28:31], a[32:35], v[44:47], v[28:31]\n"
    "v_max_u32 v11, v11, v55\n"
    "v_mfma_f32_16x16x32_bf16 v[32:35], a[36:39], v[44:47], v[32:35]\n"
    "v_cmp_eq_u32 vcc, -1, v11\n"
    "s_cmp_lg_u64 vcc, 0\n"
    "s_cbranch_scc1 L_o1_slow%=\n"
    "L_o1_done%=:\n"
    "v_mfma_f32_16x16x32_bf16 v[16:19], a[40:43], v[48:51], v[16:19]\n"
    "ds_read_b64_tr_b16 a[0:1], v2 offset:41088\n"
    "ds_read_b64_tr_b16 a[2:3], v2 offset:42368\n"
    "v_mfma_f32_16x16x32_bf16 v[20:23], a[44:47], v[48:51], v[20:23]\n"
    "ds_read_b64_tr_b16 a[4:5], v2 offset:46080\n"
    "ds_read_b64_tr_b16 a[6:7], v2 offset:47360\n"
    "v_mfma_f32_16x16x32_bf16 v[24:27], a[48:51], v[48:51], v[24:27]\n"
    "ds_read_b64_tr_b16 a[8:9], v2 offset:46112\n"
    "ds_read_b64_tr_b16 a[10:11], v2 offset:47392\n"
    "v_mfma_f32_16x16x32_bf16 v[28:31], a[52:55], v[48:51], v[28:31]\n"
    "ds_read_b64_tr_b16 a[12:13], v2 offset:46144\n"
    "ds_read_b64_tr_b16 a[14:15], v2 offset:47424\n"
    "v_mfma_f32_16x16x32_bf16 v[32:35], a[56:59], v[48:51], v[32:35]\n"
    "ds_read_b64_tr_b16 a[16:17], v2 offset:46176\n"
    "ds_read_b64_tr_b16 a[18:19], v2 offset:47456\n"
    "s_waitcnt vmcnt(4)\n"
    "v_mfma_f32_16x16x32_bf16 v[16:19], a[60:63], v[52:55], v[16:19]\n"
    "v_max3_u32 v11, v56, v57, v58\n"
    "v_mfma_f32_16x16x32_bf16 v[20:23], a[64:67], v[52:55], v[20:23]\n"
    "v_max3_u32 v11, v11, v59, v60\n"
    "v_mfma_f32_16x16x32_bf16 v[24:27], a[68:71], v[52:55], v[24:27]\n"
    "v_max3_u32 v11, v11, v61, v62\n"
    "v_mfma_f32_16x16x32_bf16 v[28:31], a[72:75], v[52:55], v[28:31]\n"
    "v_max_u32 v11, v11, v63\n"
    "v_mfma_f32_16x16x32_bf16 v[32:35], a[76:79], v[52:55], v[32:35]\n"
    "v_cmp_eq_u32 vcc, -1, v11\n"
    "s_cmp_lg_u64 vcc, 0\n"
    "s_cbranch_scc1 L_o2_slow%=\n"
    "L_o2_done%=:\n"
    "v_mfma_f32_16x16x32_bf16 v[16:19], a[80:83], v[56:59], v[16:19]\n"
    "ds_read_b64_tr_b16 a[20:21], v2 offset:46208\n"
    "ds_read_b64_tr_b16 a[22:23], v2 offset:47488\n"
    "v_mfma_f32_16x16x32_bf16 v[20:23], a[84:87], v[56:59], v[20:23]\n"
    "v_mfma_f32_16x16x32_bf16 v[24:27], a[88:91], v[56:59], v[24:27]\n"
    "v_mfma_f32_16x16x32_bf16 v[28:31], a[92:95], v[56:59], v[28:31]\n"
    "v_mfma_f32_16x16x32_bf16 v[32:35], a[96:99], v[56:59], v[32:35]\n"
    "s_waitcnt vmcnt(2)\n"
    "v_mfma_f32_16x16x32_bf16 v[16:19], a[100:103], v[60:63], v[16:19]\n"
    "v_max3_u32 v11, v64, v65, v66\n"
    "v_mfma_f32_16x16x32_bf16 v[20:23], a[104:107], v[60:63], v[20:23]\n"
    "v_max3_u32 v11, v11, v67, v68\n"
    "v_mfma_f32_16x16x32_bf16 v[24:27], a[108:111], v[60:63], v[24:27]\n"
    "v_max3_u32 v11, v11, v69, v70\n"
    "v_mfma_f32_16x16x32_bf16 v[28:31], a[112:115], v[60:63], v[28:31]\n"
    "v_max_u32 v11, v11, v71\n"
    "v_mfma_f32_16x16x32_bf16 v[32:35], a[116:119], v[60:63], v[32:35]\n"
    "v_cmp_eq_u32 vcc, -1, v11\n"
    "s_cmp_lg_u64 vcc, 0\n"
    "s_cbranch_scc1 L_o3_slow%=\n"
    "L_o3_done%=:\n"
    "v_mfma_f32_16x16x32_bf16 v[16:19], a[120:123], v[64:67], v[16:19]\n"
    "v_mfma_f32_16x16x32_bf16 v[20:23], a[124:127], v[64:67], v[20:23]\n"
    "v_mfma_f32_16x16x32_bf16 v[24:27], v[80:83], v[64:67], v[24:27]\n"
    "v_mfma_f32_16x16x32_bf16 v[28:31], v[84:87], v[64:67], v[28:31]\n"
    "v_mfma_f32_16x16x32_bf16 v[32:35], v[88:91], v[64:67], v[32:35]\n"
    "s_waitcnt vmcnt(0)\n"
    "v_mfma_f32_16x16x32_bf16 v[16:19], v[92:95], v[68:71], v[16:19]\n"
    "v_max3_u32 v11, v72, v73, v74\n"
    "v_mfma_f32_16x16x32_bf16 v[20:23], v[96:99], v[68:71], v[20:23]\n"
    "v_max3_u32 v11, v11, v75, v76\n"
    "v_mfma_f32_16x16x32_bf16 v[24:27], v[100:103], v[68:71], v[24:27]\n"
    "v_max3_u32 v11, v11, v77, v78\n"
    "v_mfma_f32_16x16x32_bf16 v[28:31], v[104:107], v[68:71], v[28:31]\n"
    "v_max_u32 v11, v11, v79\n"
    "v_mfma_f32_16x16x32_bf16 v[32:35], v[108:111], v[68:71], v[32:35]\n"
    "v_cmp_eq_u32 vcc, -1, v11\n"
    "s_cmp_lg_u64 vcc, 0\n"
    "s_cbranch_scc1 L_o4_slow%=\n"
    "L_o4_done%=:\n"
    "v_mfma_f32_16x16x32_bf16 v[16:19], v[112:115], v[72:75], v[16:19]\n"
    "v_mfma_f32_16x16x32_bf16 v[20:23], v[116:119], v[72:75], v[20:23]\n"
    "v_mfma_f32_16x16x32_bf16 v[24:27], v[120:123], v[72:75], v[24:27]\n"
    "v_mfma_f32_16x16x32_bf16 v[28:31], v[124:127], v[72:75], v[28:31]\n"
    "s_waitcnt lgkmcnt(10)\n"
    "v_mfma_f32_16x16x32_bf16 v[32:35], a[0:3], v[72:75], v[32:35]\n"
    "s_waitcnt lgkmcnt(8)\n"
    "v_mfma_f32_16x16x32_bf16 v[16:19], a[4:7], v[76:79], v[16:19]\n"
    "s_waitcnt lgkmcnt(6)\n"
    "v_mfma_f32_16x16x32_bf16 v[20:23], a[8:11], v[76:79], v[20:23]\n"
    "s_waitcnt lgkmcnt(4)\n"
    "v_mfma_f32_16x16x32_bf16 v[24:27], a[12:15], v[76:79], v[24:27]\n"
    "s_waitcnt lgkmcnt(2)\n"
    "v_mfma_f32_16x16x32_bf16 v[28:31], a[16:19], v[76:79], v[28:31]\n"
    "s_waitcnt lgkmcnt(0)\n"
    "v_mfma_f32_16x16x32_bf16 v[32:35], a[20:23], v[76:79], v[32:35]\n"
    "s_cmp_eq_u32 s91, 0\n"
    "s_cbranch_scc1 L_XA%=\n"
    "ds_write_b128 v5, v[16:19] offset:0\n"
    "ds_write_b128 v5, v[20:23] offset:1024\n"
    "s_nop 3\n"
    "ds_write_b128 v5, v[32:35] offset:4096\n"
    "s_waitcnt lgkmcnt(0)\n"
    "s_barrier\n"
    "ds_read_b128 v[60:63], v5 offset:2048\n"
    "ds_read_b128 v[64:67], v5 offset:3072\n"
    "s_waitcnt lgkmcnt(1)\n"
    "v_pk_add_f32 v[24:25], v[24:25], v[60:61]\n"
    "v_pk_add_f32 v[26:27], v[26:27], v[62:63]\n"
    "s_waitcnt lgkmcnt(0)\n"
    "v_pk_add_f32 v[28:29], v[28:29], v[64:65]\n"
    "v_pk_add_f32 v[30:31], v[30:31], v[66:67]\n"
    "v_cvt_pk_bf16_f32 v40, v24, v25\n"
    "v_cvt_pk_bf16_f32 v41, v26, v27\n"
    "v_cvt_pk_bf16_f32 v42, v28, v29\n"
    "v_cvt_pk_bf16_f32 v43, v30, v31\n"
    "s_nop 0\n"
    "v_permlane16_swap_b32 v40, v42\n"
    "v_permlane16_swap_b32 v41, v43\n"
    "buffer_store_dwordx4 v[40:43], v3, s[48:51], s14 offen offset:64\n"
    "s_endpgm\n"
    "L_XA%=:\n"
    "s_nop 1\n"
    "ds_write_b128 v5, v[24:27] offset:2048\n"
    "s_nop 0\n"
    "ds_write_b128 v5, v[28:31] offset:3072\n"
    "s_waitcnt lgkmcnt(0)\n"
    "s_barrier\n"
    "ds_read_b128 v[52:55], v5 offset:0\n"
    "ds_read_b128 v[56:59], v5 offset:1024\n"
    "ds_read_b128 v[68:71], v5 offset:4096\n"
    "s_or_b32 s15, s18, s4\n"
    "s_cmp_eq_u32 s15, 0\n"
    "s_cselect_b32 s15, 0, 0x7ffffff0\n"
    "v_mov_b32 v12, s76\n"
    "v_mov_b32 v13, s77\n"
    "s_waitcnt lgkmcnt(2)\n"
    "v_pk_add_f32 v[16:17], v[16:17], v[52:53]\n"
    "v_pk_add_f32 v[18:19], v[18:19], v[54:55]\n"
    "s_waitcnt lgkmcnt(1)\n"
    "v_pk_add_f32 v[20:21], v[20:21], v[56:57]\n"
    "v_pk_add_f32 v[22:23], v[22:23], v[58:59]\n"
    "v_cvt_pk_bf16_f32 v40, v16, v17\n"
    "v_cvt_pk_bf16_f32 v41, v18, v19\n"
    "v_cvt_pk_bf16_f32 v42, v20, v21\n"
    "v_cvt_pk_bf16_f32 v43, v22, v23\n"
    "s_nop 0\n"
    "v_permlane16_swap_b32 v40, v42\n"
    "v_permlane16_swap_b32 v41, v43\n"
    "s_waitcnt lgkmcnt(0)\n"
    "v_pk_add_f32 v[32:33], v[32:33], v[68:69]\n"
    "v_pk_add_f32 v[34:35], v[34:35], v[70:71]\n"
    "buffer_store_dwordx4 v[40:43], v3, s[48:51], s14 offen\n"
    "v_cvt_pk_bf16_f32 v44, v32, v33\n"
    "v_cvt_pk_bf16_f32 v45, v34, v35\n"
    "buffer_store_dwordx2 v[44:45], v4, s[48:51], s14 offen offset:128\n"
    "buffer_store_dwordx2 v[12:13], v15, s[36:39], s15 offen sc1\n"
    "s_endpgm\n"
    "L_o0_slow%=:\n"
    "s_mov_b32 s19, 262144\n"
    "L_o0_loop%=:\n"
    "buffer_load_dwordx4 v[40:43], v14, s[36:39], s65 offen sc1 \n"
    "buffer_load_dwordx4 v[44:47], v14, s[36:39], s66 offen sc1 \n"
    "buffer_load_dwordx4 v[48:51], v14, s[36:39], s67 offen sc1 \n"
    "buffer_load_dwordx4 v[52:55], v14, s[36:39], s68 offen sc1 \n"
    "buffer_load_dwordx4 v[56:59], v14, s[36:39], s69 offen sc1 \n"
    "buffer_load_dwordx4 v[60:63], v14, s[36:39], s70 offen sc1 \n"
    "buffer_load_dwordx4 v[64:67], v14, s[36:39], s71 offen sc1 \n"
    "buffer_load_dwordx4 v[68:71], v14, s[36:39], s72 offen sc1 \n"
    "buffer_load_dwordx4 v[72:75], v14, s[36:39], s73 offen sc1 \n"
    "buffer_load_dwordx4 v[76:79], v14, s[36:39], s74 offen sc1 \n"
    "s_waitcnt vmcnt(8)\n"
    "v_max3_u32 v11, v40, v41, v42\n"
    "v_max3_u32 v11, v11, v43, v44\n"
    "v_max3_u32 v11, v11, v45, v46\n"
    "v_max_u32 v11, v11, v47\n"
    "v_cmp_eq_u32 vcc, -1, v11\n"
    "s_cmp_lg_u64 vcc, 0\n"
    "s_cbranch_scc0 L_o0_done%=\n"
    "s_sub_u32 s19, s19, 1\n"
    "s_cbranch_scc0 L_o0_loop%=\n"
    "s_branch L_o0_done%=\n"
    "L_o1_slow%=:\n"
    "s_mov_b32 s19, 262144\n"
    "L_o1_loop%=:\n"
    "buffer_load_dwordx4 v[48:51], v14, s[36:39], s67 offen sc1 \n"
    "buffer_load_dwordx4 v[52:55], v14, s[36:39], s68 offen sc1 \n"
    "buffer_load_dwordx4 v[56:59], v14, s[36:39], s69 offen sc1 \n"
    "buffer_load_dwordx4 v[60:63], v14, s[36:39], s70 offen sc1 \n"
    "buffer_load_dwordx4 v[64:67], v14, s[36:39], s71 offen sc1 \n"
    "buffer_load_dwordx4 v[68:71], v14, s[36:39], s72 offen sc1 \n"
    "buffer_load_dwordx4 v[72:75], v14, s[36:39], s73 offen sc1 \n"
    "buffer_load_dwordx4 v[76:79], v14, s[36:39], s74 offen sc1 \n"
    "s_waitcnt vmcnt(6)\n"
    "v_max3_u32 v11, v48, v49, v50\n"
    "v_max3_u32 v11, v11, v51, v52\n"
    "v_max3_u32 v11, v11, v53, v54\n"
    "v_max_u32 v11, v11, v55\n"
    "v_cmp_eq_u32 vcc, -1, v11\n"
    "s_cmp_lg_u64 vcc, 0\n"
    "s_cbranch_scc0 L_o1_done%=\n"
    "s_sub_u32 s19, s19, 1\n"
    "s_cbranch_scc0 L_o1_loop%=\n"
    "s_branch L_o1_done%=\n"
    "L_o2_slow%=:\n"
    "s_mov_b32 s19, 262144\n"
    "L_o2_loop%=:\n"
    "buffer_load_dwordx4 v[56:59], v14, s[36:39], s69 offen sc1 \n"
    "buffer_load_dwordx4 v[60:63], v14, s[36:39], s70 offen sc1 \n"
    "buffer_load_dwordx4 v[64:67], v14, s[36:39], s71 offen sc1 \n"
    "buffer_load_dwordx4 v[68:71], v14, s[36:39], s72 offen sc1 \n"
    "buffer_load_dwordx4 v[72:75], v14, s[36:39], s73 offen sc1 \n"
    "buffer_load_dwordx4 v[76:79], v14, s[36:39], s74 offen sc1 \n"
    "s_waitcnt vmcnt(4)\n"
    "v_max3_u32 v11, v56, v57, v58\n"
    "v_max3_u32 v11, v11, v59, v60\n"
    "v_max3_u32 v11, v11, v61, v62\n"
    "v_max_u32 v11, v11, v63\n"
    "v_cmp_eq_u32 vcc, -1, v11\n"
    "s_cmp_lg_u64 vcc, 0\n"
    "s_cbranch_scc0 L_o2_done%=\n"
    "s_sub_u32 s19, s19, 1\n"
    "s_cbranch_scc0 L_o2_loop%=\n"
    "s_branch L_o2_done%=\n"
    "L_o3_slow%=:\n"
    "s_mov_b32 s19, 262144\n"
    "L_o3_loop%=:\n"
    "buffer_load_dwordx4 v[64:67], v14, s[36:39], s71 offen sc1 \n"
    "buffer_load_dwordx4 v[68:71], v14, s[36:39], s72 offen sc1 \n"
    "buffer_load_dwordx4 v[72:75], v14, s[36:39], s73 offen sc1 \n"
    "buffer_load_dwordx4 v[76:79], v14, s[36:39], s74 offen sc1 \n"
    "s_waitcnt vmcnt(2)\n"
    "v_max3_u32 v11, v64, v65, v66\n"
    "v_max3_u32 v11, v11, v67, v68\n"
    "v_max3_u32 v11, v11, v69, v70\n"
    "v_max_u32 v11, v11, v71\n"
    "v_cmp_eq_u32 vcc, -1, v11\n"
    "s_cmp_lg_u64 vcc, 0\n"
    "s_cbranch_scc0 L_o3_done%=\n"
    "s_sub_u32 s19, s19, 1\n"
    "s_cbranch_scc0 L_o3_loop%=\n"
    "s_branch L_o3_done%=\n"
    "L_o4_slow%=:\n"
    "s_mov_b32 s19, 262144\n"
    "L_o4_loop%=:\n"
    "buffer_load_dwordx4 v[72:75], v14, s[36:39], s73 offen sc1 \n"
    "buffer_load_dwordx4 v[76:79], v14, s[36:39], s74 offen sc1 \n"
    "s_waitcnt vmcnt(0)\n"
    "v_max3_u32 v11, v72, v73, v74\n"
    "v_max3_u32 v11, v11, v75, v76\n"
    "v_max3_u32 v11, v11, v77, v78\n"
    "v_max_u32 v11, v11, v79\n"
    "v_cmp_eq_u32 vcc, -1, v11\n"
    "s_cmp_lg_u64 vcc, 0\n"
    "s_cbranch_scc0 L_o4_done%=\n"
    "s_sub_u32 s19, s19, 1\n"
    "s_cbranch_scc0 L_o4_loop%=\n"
    "s_branch L_o4_done%=\n"
    "L_f1_slow%=:\n"
    "s_mov_b32 s19, 262144\n"
    "L_f1_loop%=:\n"
    "buffer_load_dwordx2 v[110:111], v8, s[36:39], 0 offen sc1 \n"
    "s_waitcnt vmcnt(0)\n"
    "v_cmp_ne_u32 s[80:81], s76, v110\n"
    "v_cmp_ne_u32 vcc, s77, v111\n"
    "s_or_b64 vcc, vcc, s[80:81]\n"
    "s_and_b32 s15, vcc_lo, 0xffffff\n"
    "s_cbranch_scc0 L_f1_done%=\n"
    "s_sub_u32 s19, s19, 1\n"
    "s_cbranch_scc0 L_f1_loop%=\n"
    "s_branch L_f1_done%=\n"
    "L_g0_slow%=:\n"
    "s_mov_b32 s19, 262144\n"
    "L_g0_loop%=:\n"
    "buffer_load_dwordx4 v[40:43], v112, s[36:39], s87 offen sc1 \n"
    "buffer_load_dwordx4 v[44:47], v113, s[36:39], s87 offen sc1 \n"
    "buffer_load_dwordx4 v[48:51], v114, s[36:39], s87 offen sc1 \n"
    "buffer_load_dwordx4 v[52:55], v115, s[36:39], s87 offen sc1 \n"
    "buffer_load_dwordx4 v[56:59], v112, s[36:39], s88 offen sc1 \n"
    "buffer_load_dwordx4 v[60:63], v113, s[36:39], s88 offen sc1 \n"
    "buffer_load_dwordx4 v[64:67], v114, s[36:39], s88 offen sc1 \n"
    "buffer_load_dwordx4 v[68:71], v115, s[36:39], s88 offen sc1 \n"
    "buffer_load_dwordx4 v[72:75], v112, s[36:39], s89 offen sc1 \n"
    "buffer_load_dwordx4 v[76:79], v113, s[36:39], s89 offen sc1 \n"
    "buffer_load_dwordx4 v[80:83], v114, s[36:39], s89 offen sc1 \n"
    "buffer_load_dwordx4 v[84:87], v115, s[36:39], s89 offen sc1 \n"
    "buffer_load_dwordx4 v[88:91], v112, s[36:39], s90 offen sc1 \n"
    "buffer_load_dwordx4 v[92:95], v113, s[36:39], s90 offen sc1 \n"
    "buffer_load_dwordx4 v[96:99], v114, s[36:39], s90 offen sc1 \n"
    "buffer_load_dwordx4 v[100:103], v115, s[36:39], s90 offen sc1 \n"
    "s_waitcnt vmcnt(0)\n"
    "v_max3_u32 v11, v40, v41, v42\n"
    "v_max3_u32 v11, v11, v43, v44\n"
    "v_max3_u32 v11, v11, v45, v46\n"
    "v_max3_u32 v11, v11, v47, v48\n"
    "v_max3_u32 v11, v11, v49, v50\n"
    "v_max3_u32 v11, v11, v51, v52\n"
    "v_max3_u32 v11, v11, v53, v54\n"
    "v_max_u32 v11, v11, v55\n"
    "v_cmp_eq_u32 vcc, -1, v11\n"
    "s_cmp_lg_u64 vcc, 0\n"
    "s_cbranch_scc0 L_g0_done%=\n"
    "s_sub_u32 s19, s19, 1\n"
    "s_cbranch_scc0 L_g0_loop%=\n"
    "s_branch L_g0_done%=\n"
    "L_g1_slow%=:\n"
    "s_mov_b32 s19, 262144\n"
    "L_g1_loop%=:\n"
    "buffer_load_dwordx4 v[56:59], v112, s[36:39], s88 offen sc1 \n"
    "buffer_load_dwordx4 v[60:63], v113, s[36:39], s88 offen sc1 \n"
    "buffer_load_dwordx4 v[64:67], v114, s[36:39], s88 offen sc1 \n"
    "buffer_load_dwordx4 v[68:71], v115, s[36:39], s88 offen sc1 \n"
    "buffer_load_dwordx4 v[72:75], v112, s[36:39], s89 offen sc1 \n"
    "buffer_load_dwordx4 v[76:79], v113, s[36:39], s89 offen sc1 \n"
    "buffer_load_dwordx4 v[80:83], v114, s[36:39], s89 offen sc1 \n"
    "buffer_load_dwordx4 v[84:87], v115, s[36:39], s89 offen sc1 \n"
    "buffer_load_dwordx4 v[88:91], v112, s[36:39], s90 offen sc1 \n"
    "buffer_load_dwordx4 v[92:95], v113, s[36:39], s90 offen sc1 \n"
    "buffer_load_dwordx4 v[96:99], v114, s[36:39], s90 offen sc1 \n"
    "buffer_load_dwordx4 v[100:103], v115, s[36:39], s90 offen sc1 \n"
    "s_waitcnt vmcnt(0)\n"
    "v_max3_u32 v11, v56, v57, v58\n"
    "v_max3_u32 v11, v11, v59, v60\n"
    "v_max3_u32 v11, v11, v61, v62\n"
    "v_max3_u32 v11, v11, v63, v64\n"
    "v_max3_u32 v11, v11, v65, v66\n"
    "v_max3_u32 v11, v11, v67, v68\n"
    "v_max3_u32 v11, v11, v69, v70\n"
    "v_max_u32 v11, v11, v71\n"
    "v_cmp_eq_u32 vcc, -1, v11\n"
    "s_cmp_lg_u64 vcc, 0\n"
    "s_cbranch_scc0 L_g1_done%=\n"
    "s_sub_u32 s19, s19, 1\n"
    "s_cbranch_scc0 L_g1_loop%=\n"
    "s_branch L_g1_done%=\n"
    "L_g2_slow%=:\n"
    "s_mov_b32 s19, 262144\n"
    "L_g2_loop%=:\n"
    "buffer_load_dwordx4 v[72:75], v112, s[36:39], s89 offen sc1 \n"
    "buffer_load_dwordx4 v[76:79], v113, s[36:39], s89 offen sc1 \n"
    "buffer_load_dwordx4 v[80:83], v114, s[36:39], s89 offen sc1 \n"
    "buffer_load_dwordx4 v[84:87], v115, s[36:39], s89 offen sc1 \n"
    "buffer_load_dwordx4 v[88:91], v112, s[36:39], s90 offen sc1 \n"
    "buffer_load_dwordx4 v[92:95], v113, s[36:39], s90 offen sc1 \n"
    "buffer_load_dwordx4 v[96:99], v114, s[36:39], s90 offen sc1 \n"
    "buffer_load_dwordx4 v[100:103], v115, s[36:39], s90 offen sc1 \n"
    "s_waitcnt vmcnt(0)\n"
    "v_max3_u32 v11, v72, v73, v74\n"
    "v_max3_u32 v11, v11, v75, v76\n"
    "v_max3_u32 v11, v11, v77, v78\n"
    "v_max3_u32 v11, v11, v79, v80\n"
    "v_max3_u32 v11, v11, v81, v82\n"
    "v_max3_u32 v11, v11, v83, v84\n"
    "v_max3_u32 v11, v11, v85, v86\n"
    "v_max_u32 v11, v11, v87\n"
    "v_cmp_eq_u32 vcc, -1, v11\n"
    "s_cmp_lg_u64 vcc, 0\n"
    "s_cbranch_scc0 L_g2_done%=\n"
    "s_sub_u32 s19, s19, 1\n"
    "s_cbranch_scc0 L_g2_loop%=\n"
    "s_branch L_g2_done%=\n"
    "L_g3_slow%=:\n"
    "s_mov_b32 s19, 262144\n"
    "L_g3_loop%=:\n"
    "buffer_load_dwordx4 v[88:91], v112, s[36:39], s90 offen sc1 \n"
    "buffer_load_dwordx4 v[92:95], v113, s[36:39], s90 offen sc1 \n"
    "buffer_load_dwordx4 v[96:99], v114, s[36:39], s90 offen sc1 \n"
    "buffer_load_dwordx4 v[100:103], v115, s[36:39], s90 offen sc1 \n"
    "s_waitcnt vmcnt(0)\n"
    "v_max3_u32 v11, v88, v89, v90\n"
    "v_max3_u32 v11, v11, v91, v92\n"
    "v_max3_u32 v11, v11, v93, v94\n"
    "v_max3_u32 v11, v11, v95, v96\n"
    "v_max3_u32 v11, v11, v97, v98\n"
    "v_max3_u32 v11, v11, v99, v100\n"
    "v_max3_u32 v11, v11, v101, v102\n"
    "v_max_u32 v11, v11, v103\n"
    "v_cmp_eq_u32 vcc, -1, v11\n"
    "s_cmp_lg_u64 vcc, 0\n"
    "s_cbranch_scc0 L_g3_done%=\n"
    "s_sub_u32 s19, s19, 1\n"
    "s_cbranch_scc0 L_g3_loop%=\n"
    "s_branch L_g3_done%=\n"
    :
    : "s"(__builtin_amdgcn_kernarg_segment_ptr()), "s"(__builtin_amdgcn_workgroup_id_x()), "v"(__builtin_amdgcn_workitem_id_x()),
      "i"((__attribute__((address_space(3))) char*)smem)
    : "v1", "v2", "v3", "v4", "v5", "v6", "v7", "v8", "v9", "v10", "v11", "v12", "v13", "v14", "v15", "v16", "v17", "v18", "v19", "v20", "v21", "v22", "v23", "v24", "v25", "v26", "v27", "v28", "v29", "v30", "v31", "v32", "v33", "v34", "v35", "v36", "v37", "v38", "v39", "v40", "v41", "v42", "v43", "v44", "v45", "v46", "v47", "v48", "v49", "v50", "v51", "v52", "v53", "v54", "v55", "v56", "v57", "v58", "v59", "v60", "v61", "v62", "v63", "v64", "v65", "v66", "v67", "v68", "v69", "v70", "v71", "v72", "v73", "v74", "v75", "v76", "v77", "v78", "v79", "v80", "v81", "v82", "v83", "v84", "v85", "v86", "v87", "v88", "v89", "v90", "v91", "v92", "v93", "v94", "v95", "v96", "v97", "v98", "v99", "v100", "v101", "v102", "v103", "v104", "v105", "v106", "v107", "v108", "v109", "v110", "v111", "v112", "v113", "v114", "v115", "v116", "v117", "v118", "v119", "v120", "v121", "v122", "v123", "v124", "v125", "v126", "v127",
      "a0", "a1", "a2", "a3", "a4", "a5", "a6", "a7", "a8", "a9", "a10", "a11", "a12", "a13", "a14", "a15", "a16", "a17", "a18", "a19", "a20", "a21", "a22", "a23", "a24", "a25", "a26", "a27", "a28", "a29", "a30", "a31", "a32", "a33", "a34", "a35", "a36", "a37", "a38", "a39", "a40", "a41", "a42", "a43", "a44", "a45", "a46", "a47", "a48", "a49", "a50", "a51", "a52", "a53", "a54", "a55", "a56", "a57", "a58", "a59", "a60", "a61", "a62", "a63", "a64", "a65", "a66", "a67", "a68", "a69", "a70", "a71", "a72", "a73", "a74", "a75", "a76", "a77", "a78", "a79", "a80", "a81", "a82", "a83", "a84", "a85", "a86", "a87", "a88", "a89", "a90", "a91", "a92", "a93", "a94", "a95", "a96", "a97", "a98", "a99", "a100", "a101", "a102", "a103", "a104", "a105", "a106", "a107", "a108", "a109", "a110", "a111", "a112", "a113", "a114", "a115", "a116", "a117", "a118", "a119", "a120", "a121", "a122", "a123", "a124", "a125", "a126", "a127",
      "s4", "s5", "s6", "s7", "s8", "s9", "s10", "s11", "s12", "s13", "s14", "s15", "s16", "s17", "s18", "s19", "s20", "s21", "s22", "s23", "s24", "s25", "s26", "s27", "s28", "s29", "s30", "s31", "s33", "s34", "s35", "s36", "s37", "s38", "s39", "s40", "s41", "s42", "s43", "s44", "s45", "s46", "s47", "s48", "s49", "s50", "s51", "s52", "s53", "s54", "s55", "s56", "s57", "s58", "s59", "s60", "s61", "s62", "s63", "s64", "s65", "s66", "s67", "s68", "s69", "s70", "s71", "s72", "s73", "s74", "s75", "s76", "s77", "s78", "s79", "s80", "s81", "s82", "s83", "s84", "s85", "s86", "s87", "s88", "s89", "s90", "s91", "s92", "s93", "s94", "s95", "s96", "vcc", "scc", "memory");
#endif
}

extern "C" void kernel_launch(void* const* d_in, const int* in_sizes, int n_in, void* d_out, int out_size, void* d_ws, size_t ws_size,
                              hipStream_t stream) {
  (void)in_sizes; (void)n_in; (void)out_size; (void)ws_size;
  hedgehog_fused<<<256, 512, 0, stream>>>(d_in[0], d_in[1], d_in[2], d_in[3], d_out, d_ws);
}
```
